```python
import torch

def _softmax(x, axis=-1):
    return torch.softmax(x, dim=axis)
def _gelu(x):
    return torch.nn.functional.gelu(x, approximate="tanh")
def _silu(x):
    return torch.nn.functional.silu(x)
def _sigmoid(x):
    return torch.sigmoid(x)
def _layernorm(x, axis=-1, eps=1e-5):
    m = x.mean(dim=axis, keepdim=True)
    v = x.var(dim=axis, keepdim=True, unbiased=False)
    return (x - m) / torch.sqrt(v + eps)
def _scatter_add(tgt, idx, src, axis=0):
    return tgt.clone().index_add_(axis, idx, src)
def _segment_sum(src, seg, n):
    out = torch.zeros((n,) + src.shape[1:], dtype=src.dtype)
    return out.index_add_(0, seg, src)
def _pad(x, pw):
    flat = tuple(v for pair in reversed(pw) for v in pair)
    return torch.nn.functional.pad(x, flat)

kernel_name = 'sequential_preLN__gqa_block_cached__swiglu_ffn__decode'

def setup_inputs():

    _w = (lambda a: (lambda t: t.to(torch.bfloat16) if t.is_floating_point() else t)(torch.as_tensor(a)))
    def _A_setup():
        B, Sq, D = 4, 1, 512
        Hq, Hkv, Sk, Dh = 32, 8, 4096, 128
        assert Hq % Hkv == 0, (Hq, Hkv)
        return {
            "x": _w((lambda *s: torch.randn(*s, dtype=torch.bfloat16) * 0.05)(B, Sq, D)),
            "k_cache": _w((lambda *s: torch.randn(*s, dtype=torch.bfloat16) * 3.2)(B, Hkv, Sk, Dh)),
            "v_cache": _w((lambda *s: torch.randn(*s, dtype=torch.bfloat16) * 0.4)(B, Hkv, Sk, Dh)),
            "wq": _w((lambda *s: torch.randn(*s, dtype=torch.bfloat16) * 3.2)(D, Hq * Dh) * (D ** -0.5)),
            "wo": _w((lambda *s: torch.randn(*s, dtype=torch.bfloat16) * 0.4)(Hq * Dh, D) * ((Hq * Dh) ** -0.5)),
        }
    _A_in = _A_setup()

    def _B_setup():
        B, S, D, F = 4, 1, 512, 1792
        return {
            "x": _w((lambda *s: torch.randn(*s, dtype=torch.bfloat16) * 0.05)(B, S, D)),
            "w_gate": _w((lambda *s: torch.randn(*s, dtype=torch.bfloat16) * 0.05)(D, F)),
            "w_up": _w((lambda *s: torch.randn(*s, dtype=torch.bfloat16) * 0.05)(D, F)),
            "w_down": _w((lambda *s: torch.randn(*s, dtype=torch.bfloat16) * 0.05)(F, D)),
        }
    _B_in = _B_setup()

    return {
        "A_x": _A_in["x"],
        "A_k_cache": _A_in["k_cache"],
        "A_v_cache": _A_in["v_cache"],
        "A_wq": _A_in["wq"],
        "A_wo": _A_in["wo"],
        "B_x": _B_in["x"],
        "B_w_gate": _B_in["w_gate"],
        "B_w_up": _B_in["w_up"],
        "B_w_down": _B_in["w_down"],
    }

def reference(A_x, A_k_cache, A_v_cache, A_wq, A_wo, B_x, B_w_gate, B_w_up, B_w_down):
    def _A_ref(x, k_cache, v_cache, wq, wo):
        B, Sq, D = x.shape
        Hq, Dh = 32, 128
        Hkv = k_cache.shape[1]
        rep = Hq // Hkv
        q = (lambda x, *d: x.permute(*d))((x @ wq).reshape(B, Sq, Hq, Dh), 0, 2, 1, 3)
        k = (lambda x, n, axis: torch.repeat_interleave(x, n, dim=axis))(k_cache, rep, axis=1)
        v = (lambda x, n, axis: torch.repeat_interleave(x, n, dim=axis))(v_cache, rep, axis=1)
        scores = torch.einsum("bhqd,bhkd->bhqk", q, k) * (Dh ** -0.5)
        p = _softmax(scores, axis=-1)
        o = torch.einsum("bhqk,bhkd->bhqd", p, v)
        return (lambda x, *d: x.permute(*d))(o, 0, 2, 1, 3).reshape(B, Sq, Hq * Dh) @ wo

    def _B_ref(x, w_gate, w_up, w_down):
        g = _silu(x @ w_gate)
        u = x @ w_up
        return (g * u) @ w_down

    x = A_x
    h = x + _A_ref(_layernorm(x, axis=-1), A_k_cache, A_v_cache, A_wq, A_wo)
    h2 = h + _B_ref(_layernorm(h, axis=-1), B_w_gate, B_w_up, B_w_down)
    return h2
```

```cpp
#include <hip/hip_runtime.h>
#include <hip/hip_bf16.h>
#include <stdint.h>

#define ROUND_LIKE_REF 0

typedef unsigned short u16;
__device__ __forceinline__ float bf2f(u16 v) { return __uint_as_float(((unsigned)v) << 16); }
__device__ __forceinline__ u16 f2bf(float f) { return __builtin_bit_cast(u16, (__bf16)f); }
__device__ __forceinline__ float rb(float f) {
  return f;
}
constexpr int B = 4, D = 512, HQ = 32, HKV = 8, SK = 4096, DH = 128, F = 1792;

__device__ void layernorm4(const float* src, float* dst, float* red) {
  int w = threadIdx.x >> 6, l = threadIdx.x & 63;
  float s = 0.f;
  for (int i = l; i < D; i += 64) s += src[w * D + i];
  for (int o = 32; o; o >>= 1) s += __shfl_xor(s, o);
  float mf = s / D;
  float v = 0.f;
  for (int i = l; i < D; i += 64) { float d = src[w * D + i] - mf; v += d * d; }
  for (int o = 32; o; o >>= 1) v += __shfl_xor(v, o);
  float m = rb(mf);
  float var = rb(v / D);
  float sd = rb(sqrtf(rb(var + 1e-5f)));
  for (int i = l; i < D; i += 64) dst[w * D + i] = rb(rb(src[w * D + i] - m) / sd);
}

__global__ void __launch_bounds__(256) k_q(const u16* x, const u16* wq, float* q) {
  __shared__ float xs[B * D], xn[B * D], red[256 * 4];
  for (int i = threadIdx.x; i < B * D; i += 256) xs[i] = bf2f(x[i]);
  __syncthreads();
  layernorm4(xs, xn, red);
  __syncthreads();
  int col = blockIdx.x * 16 + (threadIdx.x & 15), ks = threadIdx.x >> 4;
  float a[4] = {0, 0, 0, 0};
  for (int k = ks; k < D; k += 16) {
    float w = bf2f(wq[(size_t)k * 4096 + col]);
    for (int b = 0; b < 4; b++) a[b] += xn[b * D + k] * w;
  }
  for (int b = 0; b < 4; b++) red[b * 256 + threadIdx.x] = a[b];
  __syncthreads();
  if (threadIdx.x < 64) {
    int b = threadIdx.x >> 4, c = threadIdx.x & 15;
    float s = 0;
    for (int i = 0; i < 16; i++) s += red[b * 256 + i * 16 + c];
    q[b * 4096 + blockIdx.x * 16 + c] = rb(s);
  }
}

__global__ void __launch_bounds__(256) k_s(const float* q, const u16* kc, float* s) {
  int j = blockIdx.x & 7, hkv = (blockIdx.x >> 3) & 7, b = blockIdx.x >> 6;
  __shared__ float qs[512];
  for (int i = threadIdx.x; i < 512; i += 256) qs[i] = q[b * 4096 + hkv * 512 + i];
  __syncthreads();
  int c = threadIdx.x & 15, r = threadIdx.x >> 4;
  for (int t = r; t < 512; t += 16) {
    int key = j * 512 + t;
    uint4 kv = *(const uint4*)(kc + (((size_t)b * HKV + hkv) * SK + key) * DH + c * 8);
    unsigned kk[4] = {kv.x, kv.y, kv.z, kv.w};
    float a[4] = {0, 0, 0, 0};
    for (int e = 0; e < 4; e++) {
      float k0 = __uint_as_float(kk[e] << 16), k1 = __uint_as_float(kk[e] & 0xffff0000u);
      for (int h = 0; h < 4; h++) a[h] += qs[h * 128 + c * 8 + 2 * e] * k0 + qs[h * 128 + c * 8 + 2 * e + 1] * k1;
    }
    for (int h = 0; h < 4; h++) for (int o = 8; o; o >>= 1) a[h] += __shfl_xor(a[h], o);
    if (c < 4) {
      float v = c == 0 ? a[0] : c == 1 ? a[1] : c == 2 ? a[2] : a[3];
      s[((size_t)b * HQ + hkv * 4 + c) * SK + key] = rb(rb(v) * 0.08838834764831845f);
    }
  }
}
__global__ void __launch_bounds__(256) k_sm(float* s) {
  float* r = s + (size_t)blockIdx.x * SK;
  __shared__ float red[8];
  float m = -INFINITY;
  for (int i = threadIdx.x; i < SK; i += 256) m = fmaxf(m, r[i]);
  for (int o = 32; o; o >>= 1) m = fmaxf(m, __shfl_xor(m, o));
  if ((threadIdx.x & 63) == 0) red[threadIdx.x >> 6] = m;
  __syncthreads();
  m = fmaxf(fmaxf(red[0], red[1]), fmaxf(red[2], red[3]));
  float l = 0;
  for (int i = threadIdx.x; i < SK; i += 256) l += expf(r[i] - m);
  for (int o = 32; o; o >>= 1) l += __shfl_xor(l, o);
  if ((threadIdx.x & 63) == 0) red[4 + (threadIdx.x >> 6)] = l;
  __syncthreads();
  l = red[4] + red[5] + red[6] + red[7];
  for (int i = threadIdx.x; i < SK; i += 256) r[i] = rb(expf(r[i] - m) / l);
}
__global__ void __launch_bounds__(256) k_pv(const float* p, const u16* vc, float* op) {
  int j = blockIdx.x & 7, hkv = (blockIdx.x >> 3) & 7, b = blockIdx.x >> 6;
  __shared__ float ps[4 * 512];
  __shared__ float red[16 * 512];
  for (int i = threadIdx.x; i < 2048; i += 256) ps[i] = p[((size_t)b * HQ + hkv * 4 + (i >> 9)) * SK + j * 512 + (i & 511)];
  __syncthreads();
  int c = threadIdx.x & 15, r = threadIdx.x >> 4;
  float a[4][8] = {};
  for (int t = r; t < 512; t += 16) {
    int key = j * 512 + t;
    uint4 kv = *(const uint4*)(vc + (((size_t)b * HKV + hkv) * SK + key) * DH + c * 8);
    unsigned kk[4] = {kv.x, kv.y, kv.z, kv.w};
    for (int e = 0; e < 4; e++) {
      float k0 = __uint_as_float(kk[e] << 16), k1 = __uint_as_float(kk[e] & 0xffff0000u);
      for (int h = 0; h < 4; h++) { a[h][2 * e] += ps[h * 512 + t] * k0; a[h][2 * e + 1] += ps[h * 512 + t] * k1; }
    }
  }
  for (int h = 0; h < 4; h++) for (int e = 0; e < 8; e++) red[r * 512 + h * 128 + c * 8 + e] = a[h][e];
  __syncthreads();
  for (int i = threadIdx.x; i < 512; i += 256) {
    float s = 0;
    for (int rr = 0; rr < 16; rr++) s += red[rr * 512 + i];
    op[((size_t)j * B + b) * 4096 + hkv * 512 + i] = s;
  }
}
__global__ void __launch_bounds__(256) k_wo(const float* op, const u16* wo, const u16* x, float* h) {
  __shared__ float o[B * 4096];
  __shared__ float red[4 * 256];
  for (int i = threadIdx.x; i < B * 4096; i += 256) {
    float s = 0;
    for (int j = 0; j < 8; j++) s += op[(size_t)j * B * 4096 + i];
    o[i] = rb(s);
  }
  __syncthreads();
  int col = blockIdx.x * 16 + (threadIdx.x & 15), ks = threadIdx.x >> 4;
  float a[4] = {0, 0, 0, 0};
  for (int k = ks; k < 4096; k += 16) {
    float w = bf2f(wo[(size_t)k * D + col]);
    for (int b = 0; b < 4; b++) a[b] += o[b * 4096 + k] * w;
  }
  for (int b = 0; b < 4; b++) red[b * 256 + threadIdx.x] = a[b];
  __syncthreads();
  if (threadIdx.x < 64) {
    int b = threadIdx.x >> 4, c = threadIdx.x & 15;
    float s = 0;
    for (int i = 0; i < 16; i++) s += red[b * 256 + i * 16 + c];
    int cc = blockIdx.x * 16 + c;
    h[b * D + cc] = rb(bf2f(x[b * D + cc]) + rb(s));
  }
}
__global__ void __launch_bounds__(256) k_gu(const float* h, const u16* wg, const u16* wu, float* act) {
  __shared__ float xs[B * D], xn[B * D], red[256 * 8];
  for (int i = threadIdx.x; i < B * D; i += 256) xs[i] = h[i];
  __syncthreads();
  layernorm4(xs, xn, red);
  __syncthreads();
  int col = blockIdx.x * 16 + (threadIdx.x & 15), ks = threadIdx.x >> 4;
  float a[4] = {0, 0, 0, 0}, u[4] = {0, 0, 0, 0};
  for (int k = ks; k < D; k += 16) {
    float w = bf2f(wg[(size_t)k * F + col]), w2 = bf2f(wu[(size_t)k * F + col]);
    for (int b = 0; b < 4; b++) { a[b] += xn[b * D + k] * w; u[b] += xn[b * D + k] * w2; }
  }
  for (int b = 0; b < 4; b++) { red[b * 256 + threadIdx.x] = a[b]; red[(4 + b) * 256 + threadIdx.x] = u[b]; }
  __syncthreads();
  if (threadIdx.x < 64) {
    int b = threadIdx.x >> 4, c = threadIdx.x & 15;
    float s = 0, t = 0;
    for (int i = 0; i < 16; i++) { s += red[b * 256 + i * 16 + c]; t += red[(4 + b) * 256 + i * 16 + c]; }
    s = rb(s); t = rb(t);
    float g = rb(s / (1.f + expf(-s)));
    act[b * F + blockIdx.x * 16 + c] = rb(g * t);
  }
}
__global__ void __launch_bounds__(256) k_dn(const float* act, const u16* wd, const float* h, u16* out) {
  __shared__ float o[B * F];
  __shared__ float red[4 * 256];
  for (int i = threadIdx.x; i < B * F; i += 256) o[i] = act[i];
  __syncthreads();
  int col = blockIdx.x * 16 + (threadIdx.x & 15), ks = threadIdx.x >> 4;
  float a[4] = {0, 0, 0, 0};
  for (int k = ks; k < F; k += 16) {
    float w = bf2f(wd[(size_t)k * D + col]);
    for (int b = 0; b < 4; b++) a[b] += o[b * F + k] * w;
  }
  for (int b = 0; b < 4; b++) red[b * 256 + threadIdx.x] = a[b];
  __syncthreads();
  if (threadIdx.x < 64) {
    int b = threadIdx.x >> 4, c = threadIdx.x & 15;
    float s = 0;
    for (int i = 0; i < 16; i++) s += red[b * 256 + i * 16 + c];
    int cc = blockIdx.x * 16 + c;
    out[b * D + cc] = f2bf(h[b * D + cc] + rb(s));
  }
}

extern "C" void kernel_launch(void* const* d_in, const int* in_sizes, int n_in, void* d_out, int out_size, void* d_ws,
                              size_t ws_size, hipStream_t stream) {
  const u16* x = (const u16*)d_in[0];
  const u16* kc = (const u16*)d_in[1];
  const u16* vc = (const u16*)d_in[2];
  const u16* wq = (const u16*)d_in[3];
  const u16* wo = (const u16*)d_in[4];
  const u16* wg = (const u16*)d_in[6];
  const u16* wu = (const u16*)d_in[7];
  const u16* wd = (const u16*)d_in[8];
  float* ws = (float*)d_ws;
  float* q = ws;
  float* s = q + 16384;
  float* op = s + 4 * 32 * 4096;
  float* h = op + 8 * 4 * 4096;
  float* act = h + 2048;
  k_q<<<256, 256, 0, stream>>>(x, wq, q);
  k_s<<<256, 256, 0, stream>>>(q, kc, s);
  k_sm<<<128, 256, 0, stream>>>(s);
  k_pv<<<256, 256, 0, stream>>>(s, vc, op);
  k_wo<<<32, 256, 0, stream>>>(op, wo, x, h);
  k_gu<<<112, 256, 0, stream>>>(h, wg, wu, act);
  k_dn<<<32, 256, 0, stream>>>(act, wd, h, (u16*)d_out);
}
```

```cpp
#include <hip/hip_runtime.h>
#include <stdint.h>

#define DIAG 0
#define USE_B0 1
#define W_AUX 2

#define GAS __attribute__((address_space(1)))
#define LAS __attribute__((address_space(3)))
typedef unsigned v4u __attribute__((ext_vector_type(4)));
typedef unsigned v2u __attribute__((ext_vector_type(2)));
typedef float v4f __attribute__((ext_vector_type(4)));
typedef float v2f __attribute__((ext_vector_type(2)));
typedef __bf16 v8bf __attribute__((ext_vector_type(8)));
typedef __bf16 v2bf __attribute__((ext_vector_type(2)));
typedef short v4s __attribute__((ext_vector_type(4)));
typedef unsigned long long u64;
typedef __amdgpu_buffer_rsrc_t rsrc_t;

constexpr int AUX_NT = 2, AUX_SC1 = 16;
constexpr unsigned SENT = 0xFFFFFFFFu;
constexpr int SPIN_CAP = 1 << 16;
constexpr float LOG2E = 1.4426950408889634f;

constexpr int WS_EPOCH = 0;
constexpr int WS_RDY = 4096;
constexpr int WS_QG = 8192;
constexpr int WS_OP = WS_QG + 131072;
constexpr int WS_ST = WS_OP + 524288;
constexpr int WS_HP = WS_ST + 8192;
constexpr int WS_FP = WS_HP + 65536;
constexpr int WS_END = WS_FP + 4 * 56 * 2048;

constexpr int L_WQ = 0;
constexpr int L_RED = 0;
constexpr int L_RED_STRIDE = 144 * 16;
constexpr int L_Q = 40960;
constexpr int L_SM = L_Q + 1024;
constexpr int L_R3 = L_SM + 512;
constexpr int L_CS = L_R3 + 4096;
constexpr int L_R4 = L_CS + 4096;
constexpr int L_S4 = L_R4 + 4096;
constexpr int L_ACT = L_S4 + 128;
constexpr int L_H = L_ACT + 512;
constexpr int L_END = L_H + 256;
constexpr int LDS_BYTES = 96 * 1024;

__device__ __forceinline__ float bflo(unsigned u) { return __uint_as_float(u << 16); }
__device__ __forceinline__ float bfhi(unsigned u) { return __uint_as_float(u & 0xffff0000u); }
__device__ __forceinline__ unsigned pk2(float a, float b) {
  v2f f = {a, b};
  return __builtin_bit_cast(unsigned, __builtin_convertvector(f, v2bf));
}
__device__ __forceinline__ float rb(float a) { return bflo(pk2(a, a)); }
template <int CTRL> __device__ __forceinline__ float dppf(float x) {
  return __int_as_float(__builtin_amdgcn_mov_dpp(__float_as_int(x), CTRL, 0xf, 0xf, true));
}
template <int CTRL> __device__ __forceinline__ unsigned dppu(unsigned x) {
  return (unsigned)__builtin_amdgcn_mov_dpp((int)x, CTRL, 0xf, 0xf, true);
}
constexpr int XOR1 = 0xB1, XOR2 = 0x4E, HMIR = 0x141, ROR4 = 0x124, ROR8 = 0x128;
__device__ __forceinline__ float xrow_sum(float x) {
  auto s = __builtin_amdgcn_permlane16_swap(__float_as_uint(x), __float_as_uint(x), false, false);
  x = __uint_as_float(s[0]) + __uint_as_float(s[1]);
  auto t = __builtin_amdgcn_permlane32_swap(__float_as_uint(x), __float_as_uint(x), false, false);
  return __uint_as_float(t[0]) + __uint_as_float(t[1]);
}
__device__ __forceinline__ float xrow_max(float x) {
  auto s = __builtin_amdgcn_permlane16_swap(__float_as_uint(x), __float_as_uint(x), false, false);
  x = fmaxf(__uint_as_float(s[0]), __uint_as_float(s[1]));
  auto t = __builtin_amdgcn_permlane32_swap(__float_as_uint(x), __float_as_uint(x), false, false);
  return fmaxf(__uint_as_float(t[0]), __uint_as_float(t[1]));
}
__device__ __forceinline__ float sum8(float x) {
  x += dppf<XOR1>(x); x += dppf<XOR2>(x); x += dppf<HMIR>(x); return x;
}
__device__ __forceinline__ v4f mfma(v4u a, v4u b, v4f c) {
  return __builtin_amdgcn_mfma_f32_16x16x32_bf16(__builtin_bit_cast(v8bf, a), __builtin_bit_cast(v8bf, b), c, 0, 0, 0);
}
__device__ __forceinline__ rsrc_t mkrs(const void* p, unsigned bytes) {
  return __builtin_amdgcn_make_buffer_rsrc((void*)p, 0, bytes, 0x00020000);
}
__device__ __forceinline__ void bar() {
  asm volatile("s_waitcnt lgkmcnt(0)\n\ts_barrier" ::: "memory");
}
__device__ __forceinline__ unsigned canon(float f) { return min(__float_as_uint(f), 0xFFFFFFFEu); }
__device__ __forceinline__ v4u canon4(v4f f) { return v4u{canon(f.x), canon(f.y), canon(f.z), canon(f.w)}; }
__device__ __forceinline__ v4f poll16(rsrc_t rs, int off) {
  v4u v;
  for (int spin = 0; spin < SPIN_CAP; ++spin) {
    asm volatile("" ::: "memory");
    v = __builtin_amdgcn_raw_buffer_load_b128(rs, off, 0, AUX_SC1);
    const unsigned mx = max(max(v.x, v.y), max(v.z, v.w));
    if (__builtin_amdgcn_ballot_w64(mx == SENT) == 0) break;
  }
  return __builtin_bit_cast(v4f, v);
}
__device__ __forceinline__ v4u frag2(const v2u (&w)[8], int e) {
  const unsigned sel = (e & 1) ? 0x07060302u : 0x05040100u;
  v4u f;
#pragma unroll
  for (int t = 0; t < 4; ++t) f[t] = __builtin_amdgcn_perm(w[2 * t + 1][e >> 1], w[2 * t][e >> 1], sel);
  return f;
}
__device__ __forceinline__ v4u frag4(const v4u (&w)[8], int e) {
  const unsigned sel = (e & 1) ? 0x07060302u : 0x05040100u;
  v4u f;
#pragma unroll
  for (int t = 0; t < 4; ++t) f[t] = __builtin_amdgcn_perm(w[2 * t + 1][e >> 1], w[2 * t][e >> 1], sel);
  return f;
}
__device__ __forceinline__ v4u join_halves(unsigned lo, unsigned hi, bool upper) {
  const unsigned olo = dppu<ROR8>(lo), ohi = dppu<ROR8>(hi);
  return upper ? v4u{olo, ohi, lo, hi} : v4u{lo, hi, olo, ohi};
}

#define STAMP(i)
#define STAMPW(wv, i)

__global__ void __launch_bounds__(1024, 4)
fused_block(const char* __restrict__ xp, const char* __restrict__ kc, const char* __restrict__ vc,
            const char* __restrict__ wq, const char* __restrict__ wo, const char* __restrict__ wg,
            const char* __restrict__ wu, const char* __restrict__ wd, char* __restrict__ outp, char* ws
) {
  extern __shared__ __attribute__((aligned(16))) char smem_[];
  LAS char* smem = (LAS char*)smem_;
  const int tid = threadIdx.x, lane = tid & 63;
  const int w = __builtin_amdgcn_readfirstlane(tid >> 6);
  const int gq = lane >> 4, c = lane & 15;
  const int bid = blockIdx.x;
  const int g = bid & 7, r = bid >> 3, b = r >> 3, j = r & 7;
  const bool is_ffn = r < 28;
  const int cg = g * 7 + (r >> 2), fb = r & 3;
  const int ri = g * 4 + (r - 28);
  const rsrc_t rs = mkrs(ws, WS_END);
  STAMPW(0, 0);
  u64 tag = 0;

  if (w == 0) {
    const u64 ep = __hip_atomic_load((GAS u64*)(ws + WS_EPOCH), __ATOMIC_RELAXED, __HIP_MEMORY_SCOPE_AGENT);
    const rsrc_t rx = mkrs(xp, 4096);
    const rsrc_t rq = mkrs(wq + g * 1024 + r * 32, 512 * 8192);
    v4u xr[4], wr[16];
#pragma unroll
    for (int i = 0; i < 4; ++i)
      xr[i] = __builtin_amdgcn_raw_buffer_load_b128(rx, (c & 3) * 1024 + (c >> 2) * 256 + gq * 16 + i * 64, 0, 0);
#pragma unroll
    for (int i = 0; i < 16; ++i)
      wr[i] = __builtin_amdgcn_raw_buffer_load_b128(rq, (lane >> 1) * 8192 + (lane & 1) * 16, i * 32 * 8192, W_AUX);
    const v4u sv = {SENT, SENT, SENT, SENT};
    const int og = (b * 8 + g);
    __builtin_amdgcn_raw_buffer_store_b128(sv, rs, WS_OP + (og * 8 + j) * 2048 + lane * 16, 0, AUX_SC1);
    __builtin_amdgcn_raw_buffer_store_b128(sv, rs, WS_OP + (og * 8 + j) * 2048 + 1024 + lane * 16, 0, AUX_SC1);
    if (lane < 16) __builtin_amdgcn_raw_buffer_store_b128(sv, rs, WS_HP + og * 2048 + j * 256 + lane * 16, 0, AUX_SC1);
    if (lane < 4) __builtin_amdgcn_raw_buffer_store_b64(v2u{SENT, SENT}, rs, WS_ST + ((og * 4 + lane) * 8 + j) * 8, 0, AUX_SC1);
    if (is_ffn) {
      __builtin_amdgcn_raw_buffer_store_b128(sv, rs, WS_FP + (fb * 56 + cg) * 2048 + lane * 16, 0, AUX_SC1);
      __builtin_amdgcn_raw_buffer_store_b128(sv, rs, WS_FP + (fb * 56 + cg) * 2048 + 1024 + lane * 16, 0, AUX_SC1);
    }
    __builtin_amdgcn_sched_barrier(0);
    float xf[32];
    float s = 0.f;
#pragma unroll
    for (int i = 0; i < 4; ++i)
#pragma unroll
      for (int d = 0; d < 4; ++d) {
        xf[i * 8 + 2 * d] = bflo(xr[i][d]); xf[i * 8 + 2 * d + 1] = bfhi(xr[i][d]);
        s += xf[i * 8 + 2 * d] + xf[i * 8 + 2 * d + 1];
      }
    s += dppf<ROR4>(s); s += dppf<ROR8>(s); s = xrow_sum(s);
    const float mean = s * (1.f / 512.f);
    float vs = 0.f;
#pragma unroll
    for (int i = 0; i < 32; ++i) { const float d = xf[i] - mean; vs = fmaf(d, d, vs); }
    vs += dppf<ROR4>(vs); vs += dppf<ROR8>(vs); vs = xrow_sum(vs);
    const float mb = rb(mean);
    const float sd = rb(sqrtf(rb(rb(vs * (1.f / 512.f)) + 1e-5f)));
    const float rsd = 1.f / sd;
#pragma unroll
    for (int i = 0; i < 4; ++i)
#pragma unroll
      for (int d = 0; d < 4; ++d) {
        const unsigned t = pk2(xf[i * 8 + 2 * d] - mb, xf[i * 8 + 2 * d + 1] - mb);
        xr[i][d] = pk2(bflo(t) * rsd, bfhi(t) * rsd);
      }
#pragma unroll
    for (int i = 0; i < 16; ++i) *(LAS v4u*)(smem + L_WQ + i * 1024 + lane * 16) = wr[i];
    v4f qa[4];
#pragma unroll
    for (int q4 = 0; q4 < 4; ++q4) {
      qa[q4] = v4f{0.f, 0.f, 0.f, 0.f};
#pragma unroll
      for (int i = 0; i < 4; ++i) {
        const int s_ = q4 * 4 + i;
        const LAS char* ap = smem + L_WQ + (32 * s_ + 8 * gq + (c >> 2)) * 32 + (c & 3) * 8;
        const v2u lo = __builtin_bit_cast(v2u, __builtin_amdgcn_ds_read_tr16_b64_v4i16((LAS v4s*)ap));
        const v2u hi = __builtin_bit_cast(v2u, __builtin_amdgcn_ds_read_tr16_b64_v4i16((LAS v4s*)(ap + 128)));
        qa[q4] = mfma(xr[i], v4u{lo.x, lo.y, hi.x, hi.y}, qa[q4]);
      }
    }
    v4f qv = gq == 0 ? qa[0] : gq == 1 ? qa[1] : gq == 2 ? qa[2] : qa[3];
#pragma unroll
    for (int i = 0; i < 4; ++i) qv[i] = xrow_sum(qv[i]);
    const float qs = gq == 0 ? qv[0] : gq == 1 ? qv[1] : gq == 2 ? qv[2] : qv[3];
    tag = (ep + 1);
    const u64 gran = (tag << 16) | (pk2(qs, qs) & 0xffffu);
    asm volatile("s_waitcnt vmcnt(0)" ::: "memory");
    __builtin_amdgcn_raw_buffer_store_b64(__builtin_bit_cast(v2u, gran), rs, WS_QG + ((g * 4 + gq) * 512 + r * 16 + c) * 8, 0, AUX_SC1);
    if (lane == 0) __builtin_amdgcn_raw_buffer_store_b64(__builtin_bit_cast(v2u, tag), rs, WS_RDY + bid * 8, 0, AUX_SC1);
    STAMP(1);
  }

  const size_t kvbase = ((size_t)(b * 8 + g) * 4096 + j * 512) * 256;
  const rsrc_t rk = mkrs(kc + kvbase, 131072);
  const rsrc_t rv = mkrs(vc + kvbase, 131072);
  v4u kf[8], vr[8];
#pragma unroll
  for (int i = 0; i < 8; ++i)
    kf[i] = __builtin_amdgcn_raw_buffer_load_b128(rk, c * 256 + gq * 16 + (i & 3) * 64, w * 8192 + (i >> 2) * 4096, AUX_NT);
  v2u wor[8];
  {
    const rsrc_t ro = mkrs(wo + (size_t)g * 524288 + j * 128, 524288);
#pragma unroll
    for (int jj = 0; jj < 8; ++jj)
      wor[jj] = __builtin_amdgcn_raw_buffer_load_b64(ro, gq * 2048 + c * 8 + (jj >> 2) * 1024, w * 32768 + (jj & 3) * 8192, W_AUX);
  }
  const int hb = is_ffn ? fb : (ri >> 3);
  const int hw = is_ffn ? w : ((ri & 7) * 2 + (w & 1));
  const int hcol = 32 * hw + 8 * gq + 4 * (c >> 3);
  const v2u xres = *(const GAS v2u*)(xp + hb * 1024 + hcol * 2);
  __builtin_amdgcn_sched_barrier(0);
  asm volatile("s_barrier" ::: "memory");
  STAMPW(0, 2);
  if (w == 0) {
    v4u qg[4], rd[2];
    const unsigned tlo = (unsigned)(tag << 16), thi = (unsigned)(tag >> 16);
    for (int spin = 0; spin < SPIN_CAP; ++spin) {
      asm volatile("" ::: "memory");
#pragma unroll
      for (int i = 0; i < 4; ++i)
        qg[i] = __builtin_amdgcn_raw_buffer_load_b128(rs, WS_QG + (g * 4 + b) * 4096 + lane * 16 + i * 1024, 0, AUX_SC1);
#pragma unroll
      for (int i = 0; i < 2; ++i)
        rd[i] = __builtin_amdgcn_raw_buffer_load_b128(rs, WS_RDY + lane * 16 + i * 1024, 0, AUX_SC1);
      unsigned bad = 0;
#pragma unroll
      for (int i = 0; i < 4; ++i)
        bad |= ((qg[i].x ^ tlo) & 0xffff0000u) | (qg[i].y ^ thi) | ((qg[i].z ^ tlo) & 0xffff0000u) | (qg[i].w ^ thi);
#pragma unroll
      for (int i = 0; i < 2; ++i)
        bad |= (rd[i].x ^ (unsigned)tag) | (rd[i].y ^ (unsigned)(tag >> 32)) | (rd[i].z ^ (unsigned)tag) | (rd[i].w ^ (unsigned)(tag >> 32));
      if (__builtin_amdgcn_ballot_w64(bad != 0) == 0) break;
    }
#pragma unroll
    for (int i = 0; i < 4; ++i)
      *(LAS unsigned*)(smem + L_Q + i * 256 + lane * 4) = __builtin_amdgcn_perm(qg[i].z, qg[i].x, 0x05040100u);
    STAMP(3);
  }
#pragma unroll
  for (int jj = 0; jj < 8; ++jj)
    vr[jj] = __builtin_amdgcn_raw_buffer_load_b128(rv, gq * 1024 + c * 16 + (jj & 3) * 256, w * 8192 + (jj >> 2) * 4096, AUX_NT);
  __builtin_amdgcn_sched_barrier(0);
  bar();
  STAMPW(0, 4);
  float ex[8], mw;
  {
    v4u qf[4];
#pragma unroll
    for (int i = 0; i < 4; ++i) qf[i] = *(LAS v4u*)(smem + L_Q + (c >> 2) * 256 + (4 * i + gq) * 16);
    float sc[8];
#pragma unroll
    for (int t = 0; t < 2; ++t) {
      v4f a = {0.f, 0.f, 0.f, 0.f};
#pragma unroll
      for (int i = 0; i < 4; ++i) a = mfma(kf[t * 4 + i], qf[i], a);
#pragma unroll
      for (int d = 0; d < 2; ++d) {
        const unsigned u1 = pk2(a[2 * d], a[2 * d + 1]);
        const unsigned u2 = pk2(bflo(u1) * 0.08838834764831845f, bfhi(u1) * 0.08838834764831845f);
        sc[t * 4 + 2 * d] = bflo(u2); sc[t * 4 + 2 * d + 1] = bfhi(u2);
      }
    }
    mw = fmaxf(fmaxf(fmaxf(sc[0], sc[1]), fmaxf(sc[2], sc[3])), fmaxf(fmaxf(sc[4], sc[5]), fmaxf(sc[6], sc[7])));
    mw = xrow_max(mw);
    float lw = 0.f;
#pragma unroll
    for (int i = 0; i < 8; ++i) { ex[i] = __builtin_amdgcn_exp2f((sc[i] - mw) * LOG2E); lw += ex[i]; }
    lw = xrow_sum(lw);
    if (gq == 0 && (c & 3) == 0) *(LAS v2f*)(smem + L_SM + ((c >> 2) * 16 + w) * 8) = v2f{mw, lw};
  }
  __builtin_amdgcn_sched_barrier(0);
  v2u gur[8], dnr[8];
  if (is_ffn) {
    const char* gp = (c < 8 ? wg : wu) + (size_t)(32 * w + 8 * gq) * 3584 + cg * 64 + (c & 7) * 8;
#pragma unroll
    for (int jj = 0; jj < 8; ++jj) {
      gur[jj] = __builtin_nontemporal_load((const GAS v2u*)(gp + jj * 3584));
    }
    const rsrc_t rdn = mkrs(wd + cg * 32768, 32768);
#pragma unroll
    for (int jj = 0; jj < 8; ++jj)
      dnr[jj] = __builtin_amdgcn_raw_buffer_load_b64(rdn, gq * 8192 + c * 8 + (jj & 3) * 1024, (w & 7) * 128 + (jj >> 2) * 4096 + (w >> 3) * 32768, W_AUX);
  } else {
#pragma unroll
    for (int jj = 0; jj < 8; ++jj) { gur[jj] = v2u{0u, 0u}; dnr[jj] = v2u{0u, 0u}; }
  }
  __builtin_amdgcn_sched_barrier(0);
  bar();
  STAMPW(0, 5);
  v4u pb;
  {
    const v2f st = *(LAS v2f*)(smem + L_SM + ((c >> 2) * 16 + 4 * gq + (c & 3)) * 8);
    float M = st.x;
    M = fmaxf(M, dppf<XOR1>(M)); M = fmaxf(M, dppf<XOR2>(M)); M = xrow_max(M);
    if (w == 1) {
      float L = st.y * __builtin_amdgcn_exp2f((st.x - M) * LOG2E);
      L += dppf<XOR1>(L); L += dppf<XOR2>(L); L = xrow_sum(L);
      if (gq == 0 && (c & 3) == 0)
        __builtin_amdgcn_raw_buffer_store_b64(v2u{canon(M), canon(L)}, rs, WS_ST + (((b * 8 + g) * 4 + (c >> 2)) * 8 + j) * 8, 0, AUX_SC1);
    }
    const float f = __builtin_amdgcn_exp2f((mw - M) * LOG2E);
#pragma unroll
    for (int t = 0; t < 4; ++t) pb[t] = pk2(ex[2 * t] * f, ex[2 * t + 1] * f);
  }
  {
    v4f acc[2] = {v4f{0.f, 0.f, 0.f, 0.f}, v4f{0.f, 0.f, 0.f, 0.f}};
#pragma unroll
    for (int e4 = 0; e4 < 4; ++e4) {
      const bool on = (c & 3) == e4;
      const v4u pm = {on ? pb.x : 0u, on ? pb.y : 0u, on ? pb.z : 0u, on ? pb.w : 0u};
      acc[0] = mfma(frag4(vr, e4), pm, acc[0]);
      acc[1] = mfma(frag4(vr, 4 + e4), pm, acc[1]);
    }
    STAMPW(0, 6);
    LAS char* rp = smem + L_RED + w * L_RED_STRIDE + (36 * (c >> 2) + 8 * gq + (c & 3)) * 16;
    *(LAS v4f*)(rp) = acc[0];
    *(LAS v4f*)(rp + 64) = acc[1];
  }
  bar();
  STAMPW(0, 7);
  if (w < 2) {
    const LAS char* rp = smem + L_RED + (36 * (tid >> 5) + (tid & 31)) * 16;
    v4f s = *(LAS v4f*)(rp);
#pragma unroll
    for (int i = 1; i < 16; ++i) s += *(LAS v4f*)(rp + i * L_RED_STRIDE);
    __builtin_amdgcn_raw_buffer_store_b128(canon4(s), rs, WS_OP + ((b * 8 + g) * 8 + j) * 2048 + tid * 16, 0, AUX_SC1);
  }
  STAMPW(0, 8);
  {
    v2u st;
    for (int spin = 0; spin < SPIN_CAP; ++spin) {
      asm volatile("" ::: "memory");
      st = __builtin_amdgcn_raw_buffer_load_b64(rs, WS_ST + (((b * 8 + g) * 4 + (w >> 2)) * 8 + (c & 7)) * 8, 0, AUX_SC1);
      if (__builtin_amdgcn_ballot_w64(max(st.x, st.y) == SENT) == 0) break;
    }
    const float m = __uint_as_float(st.x), l = __uint_as_float(st.y);
    float M = m;
    M = fmaxf(M, dppf<XOR1>(M)); M = fmaxf(M, dppf<XOR2>(M)); M = fmaxf(M, dppf<HMIR>(M));
    const float wt = __builtin_amdgcn_exp2f((m - M) * LOG2E);
    const float L = sum8(l * wt);
    const float fac = wt / L;
    v4u wf[4];
#pragma unroll
    for (int e = 0; e < 4; ++e) wf[e] = frag2(wor, e);
    if (is_ffn) {
      const v4u ones = {0x3F803F80u, 0x3F803F80u, 0x3F803F80u, 0x3F803F80u};
      v4f cs;
#pragma unroll
      for (int e = 0; e < 4; ++e) cs[e] = mfma(ones, frag2(gur, e), v4f{0.f, 0.f, 0.f, 0.f}).x;
      if (gq == 0) *(LAS v4f*)(smem + L_CS + w * 256 + c * 16) = cs;
    }
    STAMPW(0, 9);
    v4f o = poll16(rs, WS_OP + ((b * 8 + g) * 8 + (c & 7)) * 2048 + w * 128 + (2 * gq + (c >> 3)) * 16);
    STAMPW(0, 10);
#pragma unroll
    for (int i = 0; i < 4; ++i) o[i] = sum8(o[i] * fac);
    const v4u of = join_halves(pk2(o.x, o.y), pk2(o.z, o.w), c >= 8);
    v4f d;
#pragma unroll
    for (int e = 0; e < 4; ++e) d[e] = mfma(of, wf[e], v4f{0.f, 0.f, 0.f, 0.f}).x;
    if (gq == 0) *(LAS v4f*)(smem + L_R3 + w * 256 + c * 16) = d;
  }
  bar();
  if (w == 15) {
    const LAS char* rp = smem + L_R3 + gq * 1024 + c * 16;
    v4f s = (*(LAS v4f*)(rp) + *(LAS v4f*)(rp + 256)) + (*(LAS v4f*)(rp + 512) + *(LAS v4f*)(rp + 768));
#pragma unroll
    for (int i = 0; i < 4; ++i) s[i] = xrow_sum(s[i]);
    if (gq == 0) __builtin_amdgcn_raw_buffer_store_b128(canon4(s), rs, WS_HP + (b * 8 + g) * 2048 + j * 256 + c * 16, 0, AUX_SC1);
    STAMP(11);
  }
  if (is_ffn) {
    v4f cs = {0.f, 0.f, 0.f, 0.f};
    if (w < 8) {
      const LAS char* rp = smem + L_CS + gq * 1024 + c * 16;
      cs = (*(LAS v4f*)(rp) + *(LAS v4f*)(rp + 256)) + (*(LAS v4f*)(rp + 512) + *(LAS v4f*)(rp + 768));
#pragma unroll
      for (int i = 0; i < 4; ++i) cs[i] = xrow_sum(cs[i]);
    }
    v4u gf[4], df[4];
#pragma unroll
    for (int e = 0; e < 4; ++e) { gf[e] = frag2(gur, e); df[e] = frag2(dnr, e); }
    v4f a = poll16(rs, WS_HP + (hb * 8 + (c & 7)) * 2048 + hcol * 4);
    STAMPW(0, 12);
#pragma unroll
    for (int i = 0; i < 4; ++i) a[i] = sum8(a[i]);
    const unsigned a0 = pk2(a.x, a.y), a1 = pk2(a.z, a.w);
    const unsigned h0 = pk2(bflo(xres.x) + bflo(a0), bfhi(xres.x) + bfhi(a0));
    const unsigned h1 = pk2(bflo(xres.y) + bflo(a1), bfhi(xres.y) + bfhi(a1));
    const v4u hf = join_halves(h0, h1, c >= 8);
    const v4u hx = (c == 1) ? v4u{0x3F803F80u, 0x3F803F80u, 0x3F803F80u, 0x3F803F80u} : hf;
    v4f d;
#pragma unroll
    for (int e = 0; e < 4; ++e) d[e] = mfma(hf, gf[e], v4f{0.f, 0.f, 0.f, 0.f}).x;
    const float stv = mfma(hf, hx, v4f{0.f, 0.f, 0.f, 0.f}).x;
    if (gq == 0) *(LAS v4f*)(smem + L_R4 + w * 256 + c * 16) = d;
    if (lane < 2) *(LAS float*)(smem + L_S4 + w * 8 + lane * 4) = stv;
    bar();
    STAMPW(0, 13);
    if (w < 8) {
      const LAS char* rp = smem + L_R4 + gq * 1024 + c * 16;
      v4f s = (*(LAS v4f*)(rp) + *(LAS v4f*)(rp + 256)) + (*(LAS v4f*)(rp + 512) + *(LAS v4f*)(rp + 768));
      v2f st = *(LAS v2f*)(smem + L_S4 + c * 8);
#pragma unroll
      for (int i = 0; i < 2; ++i) { st[i] = sum8(st[i]); st[i] += dppf<ROR8>(st[i]); }
#pragma unroll
      for (int i = 0; i < 4; ++i) s[i] = xrow_sum(s[i]);
      const float mean = st.y * (1.f / 512.f);
      const float var = fmaxf(st.x * (1.f / 512.f) - mean * mean, 0.f);
      const float mb = rb(mean);
      const float sd = rb(sqrtf(rb(rb(var) + 1e-5f)));
      const float rsd = __builtin_amdgcn_rcpf(sd);
      const unsigned y0 = pk2((s.x - mb * cs.x) * rsd, (s.y - mb * cs.y) * rsd);
      const unsigned y1 = pk2((s.z - mb * cs.z) * rsd, (s.w - mb * cs.w) * rsd);
      const unsigned u0 = dppu<ROR8>(y0), u1 = dppu<ROR8>(y1);
      float gv[4] = {bflo(y0), bfhi(y0), bflo(y1), bfhi(y1)};
#pragma unroll
      for (int i = 0; i < 4; ++i) gv[i] = gv[i] * __builtin_amdgcn_rcpf(1.f + __builtin_amdgcn_exp2f(-gv[i] * LOG2E));
      const unsigned s0 = pk2(gv[0], gv[1]), s1 = pk2(gv[2], gv[3]);
      const v2u act = {pk2(bflo(s0) * bflo(u0), bfhi(s0) * bfhi(u0)), pk2(bflo(s1) * bflo(u1), bfhi(s1) * bfhi(u1))};
      if (lane < 8) *(LAS v2u*)(smem + L_ACT + w * 64 + lane * 8) = act;
      asm volatile("s_waitcnt lgkmcnt(0)" ::: "memory");
      const v4u af = *(LAS v4u*)(smem + L_ACT + w * 64 + gq * 16);
      v4f y;
#pragma unroll
      for (int e = 0; e < 4; ++e) y[e] = mfma(af, df[e], v4f{0.f, 0.f, 0.f, 0.f}).x;
      if (gq == 0) __builtin_amdgcn_raw_buffer_store_b128(canon4(y), rs, WS_FP + (fb * 56 + cg) * 2048 + w * 256 + c * 16, 0, AUX_SC1);
      STAMPW(0, 14);
    }
  } else {
    if (w >= 14) {
      v4f a = poll16(rs, WS_HP + (hb * 8 + (c & 7)) * 2048 + hcol * 4);
#pragma unroll
      for (int i = 0; i < 4; ++i) a[i] = sum8(a[i]);
      const unsigned a0 = pk2(a.x, a.y), a1 = pk2(a.z, a.w);
      const unsigned h0 = pk2(bflo(xres.x) + bflo(a0), bfhi(xres.x) + bfhi(a0));
      const unsigned h1 = pk2(bflo(xres.y) + bflo(a1), bfhi(xres.y) + bfhi(a1));
      if ((c & 7) == 0) *(LAS v4f*)(smem + L_H + ((w & 1) * 32 + 8 * gq + 4 * (c >> 3)) * 4) = v4f{bflo(h0), bfhi(h0), bflo(h1), bfhi(h1)};
      if (gq == 0) *(LAS v4f*)(smem + L_R4 + w * 256 + c * 16) = v4f{0.f, 0.f, 0.f, 0.f};
    } else {
      v4f a = poll16(rs, WS_FP + (hb * 56 + 4 * w + gq) * 2048 + (ri & 7) * 256 + c * 16);
      STAMPW(0, 15);
#pragma unroll
      for (int i = 0; i < 4; ++i) a[i] = xrow_sum(a[i]);
      if (gq == 0) *(LAS v4f*)(smem + L_R4 + w * 256 + c * 16) = a;
    }
    bar();
    if (w == 0) {
      const LAS char* rp = smem + L_R4 + gq * 1024 + c * 16;
      v4f s = (*(LAS v4f*)(rp) + *(LAS v4f*)(rp + 256)) + (*(LAS v4f*)(rp + 512) + *(LAS v4f*)(rp + 768));
      const v4f h = *(LAS v4f*)(smem + L_H + c * 16);
#pragma unroll
      for (int i = 0; i < 4; ++i) s[i] = xrow_sum(s[i]);
      const unsigned d0 = pk2(s.x, s.y), d1 = pk2(s.z, s.w);
      const v2u o = {pk2(h.x + bflo(d0), h.y + bfhi(d0)), pk2(h.z + bflo(d1), h.w + bfhi(d1))};
      if (gq == 0) *(GAS v2u*)(outp + hb * 1024 + (ri & 7) * 128 + c * 8) = o;
      if (ri == 0 && lane == 0) __hip_atomic_store((GAS u64*)(ws + WS_EPOCH), tag, __ATOMIC_RELAXED, __HIP_MEMORY_SCOPE_AGENT);
      STAMP(16);
    }
  }
}

extern "C" void kernel_launch(void* const* d_in, const int* in_sizes, int n_in, void* d_out, int out_size, void* d_ws,
                              size_t ws_size, hipStream_t stream) {
  (void)hipFuncSetAttribute((const void*)fused_block, hipFuncAttributeMaxDynamicSharedMemorySize, LDS_BYTES);
  fused_block<<<256, 1024, LDS_BYTES, stream>>>((const char*)d_in[0], (const char*)d_in[1], (const char*)d_in[2],
                                                (const char*)d_in[3], (const char*)d_in[4], (const char*)d_in[6],
                                                (const char*)d_in[7], (const char*)d_in[8], (char*)d_out, (char*)d_ws
  );
}
```

```cpp
#include <hip/hip_runtime.h>
#include <stdint.h>

#define GAS __attribute__((address_space(1)))
#define LAS __attribute__((address_space(3)))
typedef unsigned v4u __attribute__((ext_vector_type(4)));
typedef unsigned v2u __attribute__((ext_vector_type(2)));
typedef float v4f __attribute__((ext_vector_type(4)));
typedef float v2f __attribute__((ext_vector_type(2)));
typedef __bf16 v8bf __attribute__((ext_vector_type(8)));
typedef __bf16 v2bf __attribute__((ext_vector_type(2)));
typedef short v4s __attribute__((ext_vector_type(4)));
typedef unsigned long long u64;
typedef u64 v4q __attribute__((ext_vector_type(4)));
typedef __amdgpu_buffer_rsrc_t rsrc_t;

constexpr int AUX_NT = 2, AUX_SC1 = 16;
constexpr int SPIN_CAP = 1 << 20, LDS_CAP = 1 << 25, HINT_CAP = 1 << 10;
constexpr float LOG2E = 1.4426950408889634f;
constexpr unsigned ONES = 0x3F803F80u;
constexpr unsigned OOB = 0x80000000u;
constexpr int C_DELAY = 6;

constexpr int WS_EPOCH = 0;
constexpr int WS_QG = 4096;
constexpr int WS_OP = WS_QG + 262144;
constexpr int WS_ST = WS_OP + 524288;
constexpr int WS_HP = WS_ST + 32768;
constexpr int WS_HR = WS_HP + 65536;
constexpr int WS_FP = WS_HR + 16384;
constexpr int WS_FB = WS_FP + 4 * 56 * 2048;
constexpr int WS_FC = WS_FB + 114 * 2048;
constexpr int WS_END = WS_FC + 128 * 14 * 128;

constexpr int L_A = 0;
constexpr int L_DN = 65536;
constexpr int DN_PITCH = 1056;
constexpr int L_RED = L_DN + 32 * DN_PITCH + 128;
constexpr int RED_PITCH = 2304;
constexpr int L_R = L_RED + 16 * RED_PITCH;
constexpr int L_XS = L_R;
constexpr int L_Q = L_R + 10240;
constexpr int L_SM = L_Q + 1024;
constexpr int L_S4 = L_SM + 512;
constexpr int L_FAC = L_S4 + 128;
constexpr int L_FLAG = L_FAC + 256;
constexpr int F_Q = L_FLAG, F_ST = L_FLAG + 4, F_PA = L_FLAG + 8, F_PB = L_FLAG + 12, F_A = L_FLAG + 16, F_C = L_FLAG + 20,
              F_XN = L_FLAG + 24, F_KI = L_FLAG + 28, F_VI = L_FLAG + 32, F_PUB = L_FLAG + 36, F_WI = L_FLAG + 40,
              F_WO = L_FLAG + 44, L_TAG = L_FLAG + 48;
constexpr int L_DUMP = L_FLAG + 64;
constexpr int LDS_BYTES = L_DUMP + 1024;
static_assert(LDS_BYTES <= 163840 && LDS_BYTES > 81920, "one workgroup per CU");

__device__ __forceinline__ float bflo(unsigned u) { return __uint_as_float(u << 16); }
__device__ __forceinline__ float bfhi(unsigned u) { return __uint_as_float(u & 0xffff0000u); }
__device__ __forceinline__ unsigned pk2(float a, float b) {
  v2f f = {a, b};
  return __builtin_bit_cast(unsigned, __builtin_convertvector(f, v2bf));
}
__device__ __forceinline__ float rb(float a) { return bflo(pk2(a, a)); }
__device__ __forceinline__ v4f asf(v4u v) { return __builtin_bit_cast(v4f, v); }
template <int CTRL> __device__ __forceinline__ float dppf(float x) {
  return __int_as_float(__builtin_amdgcn_mov_dpp(__float_as_int(x), CTRL, 0xf, 0xf, true));
}
template <int CTRL> __device__ __forceinline__ unsigned dppu(unsigned x) {
  return (unsigned)__builtin_amdgcn_mov_dpp((int)x, CTRL, 0xf, 0xf, true);
}
template <int CTRL, int ROWM, int BANKM> __device__ __forceinline__ unsigned dppm(unsigned old, unsigned x) {
  return (unsigned)__builtin_amdgcn_update_dpp((int)old, (int)x, CTRL, ROWM, BANKM, false);
}
constexpr int XOR1 = 0xB1, XOR2 = 0x4E, HMIR = 0x141, ROR4 = 0x124, ROR8 = 0x128, IDENT = 0xE4;
constexpr int SHR2 = 0x112, SHR4 = 0x114, SHL1 = 0x101, SHL2 = 0x102, SHL4 = 0x104, SHL6 = 0x106;
__device__ __forceinline__ float swadd32(float a, float b) {
  auto s = __builtin_amdgcn_permlane32_swap(__float_as_uint(a), __float_as_uint(b), false, false);
  return __uint_as_float(s[0]) + __uint_as_float(s[1]);
}
__device__ __forceinline__ float swadd16(float a, float b) {
  auto s = __builtin_amdgcn_permlane16_swap(__float_as_uint(a), __float_as_uint(b), false, false);
  return __uint_as_float(s[0]) + __uint_as_float(s[1]);
}
__device__ __forceinline__ float xrow_sum(float x) { x = swadd16(x, x); return swadd32(x, x); }
__device__ __forceinline__ float wave_sum(float x) {
  x += dppf<XOR1>(x); x += dppf<XOR2>(x); x += dppf<HMIR>(x); x += dppf<ROR8>(x);
  return xrow_sum(x);
}
__device__ __forceinline__ float xrow_max(float x) {
  auto s = __builtin_amdgcn_permlane16_swap(__float_as_uint(x), __float_as_uint(x), false, false);
  x = fmaxf(__uint_as_float(s[0]), __uint_as_float(s[1]));
  auto t = __builtin_amdgcn_permlane32_swap(__float_as_uint(x), __float_as_uint(x), false, false);
  return fmaxf(__uint_as_float(t[0]), __uint_as_float(t[1]));
}
__device__ __forceinline__ float rows_scatter(v4f x) { return swadd16(swadd32(x.x, x.z), swadd32(x.y, x.w)); }
__device__ __forceinline__ void rows_scatter2(v4f x, v4f y, unsigned& zx, unsigned& zy) {
  float p = swadd32(x.x, x.z), q = swadd32(x.y, x.w), r = swadd32(y.x, y.z), s = swadd32(y.y, y.w);
  asm volatile("" : "+v"(p), "+v"(q), "+v"(r), "+v"(s));
  zx = __float_as_uint(swadd16(p, q)); zy = __float_as_uint(swadd16(r, s));
}
__device__ __forceinline__ v4u pack8(float z) {
  const unsigned r0 = pk2(z, dppf<SHL1>(z));
  return v4u{r0, dppu<SHL2>(r0), dppu<SHL4>(r0), dppu<SHL6>(r0)};
}
__device__ __forceinline__ v4f mfma(v4u a, v4u b, v4f c) {
  return __builtin_amdgcn_mfma_f32_16x16x32_bf16(__builtin_bit_cast(v8bf, a), __builtin_bit_cast(v8bf, b), c, 0, 0, 0);
}
__device__ __forceinline__ v4f mfma0(v4u a, v4u b) { return mfma(a, b, v4f{0.f, 0.f, 0.f, 0.f}); }
__device__ __forceinline__ rsrc_t mkrs(const void* p, unsigned bytes) {
  return __builtin_amdgcn_make_buffer_rsrc((void*)p, 0, bytes, 0x00020000);
}
#define PIN(x) asm volatile("" : "+v"(x))

__device__ __forceinline__ void bar() { asm volatile("s_waitcnt lgkmcnt(0)\n\ts_barrier" ::: "memory"); }
template <bool SLEEP = true> __device__ __forceinline__ void lds_wait(unsigned p, unsigned want) {
#pragma nounroll
  for (int spin = 0; spin < LDS_CAP; ++spin) {
    if ((unsigned)__builtin_amdgcn_readfirstlane(*(volatile LAS unsigned*)(size_t)p) >= want) break;
    if (SLEEP) __builtin_amdgcn_s_sleep(1);
  }
  asm volatile("" ::: "memory");
}
__device__ __forceinline__ void lds_arrive(unsigned p) {
  asm volatile("s_mov_b64 exec, 1\n\tds_add_u32 %0, %1\n\ts_mov_b64 exec, -1" :: "v"(p), "v"(1u) : "memory");
}

__device__ __forceinline__ void dma_nt(const char* sbase, unsigned voff, unsigned lds_dst) {
  unsigned keep;
  asm volatile("s_mov_b32 %0, m0\n\ts_mov_b32 m0, %3\n\ts_nop 0\n\tglobal_load_lds_dwordx4 %1, %2 nt\n\ts_mov_b32 m0, %0"
               : "=&s"(keep) : "v"(voff), "s"(sbase), "s"(lds_dst) : "memory");
}
__device__ __forceinline__ void dma_l2(const char* sbase, unsigned voff, unsigned lds_dst) {
  unsigned keep;
  asm volatile("s_mov_b32 %0, m0\n\ts_mov_b32 m0, %3\n\ts_nop 0\n\tglobal_load_lds_dwordx4 %1, %2 sc1\n\ts_mov_b32 m0, %0"
               : "=&s"(keep) : "v"(voff), "s"(sbase), "s"(lds_dst) : "memory");
}
__device__ __forceinline__ v4u trfrag(unsigned p, int second) {
  const v2u lo = __builtin_bit_cast(v2u, __builtin_amdgcn_ds_read_tr16_b64_v4i16((LAS v4s*)(size_t)p));
  const v2u hi = __builtin_bit_cast(v2u, __builtin_amdgcn_ds_read_tr16_b64_v4i16((LAS v4s*)(size_t)(p + second)));
  return v4u{lo.x, lo.y, hi.x, hi.y};
}
template <typename T> __device__ __forceinline__ LAS T& lds(unsigned p) { return *(LAS T*)(size_t)p; }
__device__ __forceinline__ v4f sum2(unsigned p) { return lds<v4f>(p) + lds<v4f>(p + 1024); }

template <int BYTE> __device__ __forceinline__ unsigned tagw(float f, unsigned tag) {
  return __builtin_amdgcn_perm(__float_as_uint(f), tag, 0x07060500u + BYTE);
}
__device__ __forceinline__ unsigned or4(v4u v) { return (v.x | v.y | v.z) | v.w; }
struct Quad { v4u a, b, c, d; };
struct Src { const char *a, *b, *c, *d; };
#define Q4OPS(q) "+v"(q.a), "+v"(q.b), "+v"(q.c), "+v"(q.d)
#define S4OPS(p) "s"(p.a), "s"(p.b), "s"(p.c), "s"(p.d)
#define Q4LOADS "s_wqm_b64 exec, %9\n\tglobal_load_dwordx4 %0, %4, %5 sc1\n\tglobal_load_dwordx4 %1, %4, %6 sc1\n\t" \
                "s_wqm_b64 exec, %10\n\tglobal_load_dwordx4 %2, %4, %7 sc1\n\tglobal_load_dwordx4 %3, %4, %8 sc1\n\ts_mov_b64 exec, -1"
__device__ __forceinline__ void quad_issue(Quad& q, unsigned off, const Src& p, u64 m1) {
  asm volatile(Q4LOADS : Q4OPS(q) : "v"(off), S4OPS(p), "s"(~0ull), "s"(m1) : "memory", "scc");
}
__device__ __forceinline__ void quad_hinted(Quad& q, unsigned off, const Src& p, const char* fp, u64 t64) {
  asm volatile(
      "s_mov_b64 s[88:89], -1\n\ts_mov_b64 s[90:91], -1\n\ts_movk_i32 s92, %11\n"
      "1:\n\ts_load_dwordx4 s[76:79], %9, 0x0 glc\n\ts_load_dwordx4 s[80:83], %9, 0x80 glc\n\ts_waitcnt lgkmcnt(0)\n\t"
      "s_xor_b64 s[76:77], s[76:77], %10\n\ts_xor_b64 s[78:79], s[78:79], %10\n\ts_xor_b64 s[80:81], s[80:81], %10\n\ts_xor_b64 s[82:83], s[82:83], %10\n\t"
      "s_or_b64 s[84:85], s[76:77], s[78:79]\n\ts_or_b64 s[86:87], s[80:81], s[82:83]\n\ts_or_b64 s[84:85], s[84:85], s[86:87]\n\t"
      "s_cbranch_scc0 2f\n\t"
      "s_cmp_eq_u32 s76, 0\n\ts_cselect_b32 s84, 0xffff, 0\n\ts_cmp_eq_u32 s77, 0\n\ts_cselect_b32 s76, 0xffff0000, 0\n\ts_or_b32 s84, s84, s76\n\t"
      "s_cmp_eq_u32 s78, 0\n\ts_cselect_b32 s85, 0xffff, 0\n\ts_cmp_eq_u32 s79, 0\n\ts_cselect_b32 s76, 0xffff0000, 0\n\ts_or_b32 s85, s85, s76\n\t"
      "s_cmp_eq_u32 s80, 0\n\ts_cselect_b32 s86, 0xffff, 0\n\ts_cmp_eq_u32 s81, 0\n\ts_cselect_b32 s76, 0xffff0000, 0\n\ts_or_b32 s86, s86, s76\n\t"
      "s_cmp_eq_u32 s82, 0\n\ts_cselect_b32 s87, 0xffff, 0\n\ts_cmp_eq_u32 s83, 0\n\ts_cselect_b32 s76, 0xffff0000, 0\n\ts_or_b32 s87, s87, s76\n\t"
      "s_and_b64 exec, s[84:85], s[88:89]\n\ts_cbranch_execz 3f\n\t"
      "global_load_dwordx4 %0, %4, %5 sc1\n\tglobal_load_dwordx4 %1, %4, %6 sc1\n\ts_andn2_b64 s[88:89], s[88:89], exec\n"
      "3:\n\ts_and_b64 exec, s[86:87], s[90:91]\n\ts_cbranch_execz 4f\n\t"
      "global_load_dwordx4 %2, %4, %7 sc1\n\tglobal_load_dwordx4 %3, %4, %8 sc1\n\ts_andn2_b64 s[90:91], s[90:91], exec\n"
      "4:\n\ts_sub_u32 s92, s92, 1\n\ts_cmp_lg_u32 s92, 0\n\ts_cbranch_scc1 1b\n"
      "2:\n\ts_mov_b64 exec, s[88:89]\n\tglobal_load_dwordx4 %0, %4, %5 sc1\n\tglobal_load_dwordx4 %1, %4, %6 sc1\n\t"
      "s_mov_b64 exec, s[90:91]\n\tglobal_load_dwordx4 %2, %4, %7 sc1\n\tglobal_load_dwordx4 %3, %4, %8 sc1\n\t"
      "s_mov_b64 exec, -1"
      : Q4OPS(q) : "v"(off), S4OPS(p), "s"(fp), "s"(t64), "n"(HINT_CAP)
      : "memory", "scc", "s76", "s77", "s78", "s79", "s80", "s81", "s82", "s83", "s84", "s85", "s86", "s87", "s88", "s89", "s90", "s91", "s92");
}
__device__ __forceinline__ void quad_wait(Quad& q) { asm volatile("s_waitcnt vmcnt(0)" : Q4OPS(q) :: "memory"); }
__device__ __forceinline__ u64 quad_strip(const Quad& q, Quad& e, v4u tag) {
  e.a = q.a ^ tag; e.b = q.b ^ tag; e.c = q.c ^ tag; e.d = q.d ^ tag;
  return __builtin_amdgcn_ballot_w64((((or4(e.a) | or4(e.b)) | (or4(e.c) | or4(e.d))) & 0xFFu) != 0);
}
__device__ __forceinline__ void quad_finish(Quad& q, Quad& e, v4u tag, unsigned off, const Src& p, u64 m1) {
  u64 m = quad_strip(q, e, tag);
  if (__builtin_expect(m != 0, 0)) {
#pragma nounroll
    for (int spin = 0; spin < SPIN_CAP && m != 0; ++spin) {
      asm volatile(Q4LOADS "\n\ts_waitcnt vmcnt(0)" : Q4OPS(q) : "v"(off), S4OPS(p), "s"(m), "s"(m & m1) : "memory", "scc");
      m = quad_strip(q, e, tag);
    }
  }
}

__global__ void __launch_bounds__(1024, 4)
fused_block(const char* __restrict__ xp, const char* __restrict__ kc, const char* __restrict__ vc, const char* __restrict__ wq,
            char* ws, const char* __restrict__ wo, const char* wg_, const char* wu_, const char* wd_, char* out_) {
  extern __shared__ __attribute__((aligned(16))) char smem_[];
  const unsigned lds0 = (unsigned)(size_t)(LAS char*)smem_;
  if (threadIdx.x < 12) lds<volatile unsigned>(lds0 + L_FLAG + threadIdx.x * 4) = 0u;
  bar();
  asm volatile("" :: "s"(xp), "s"(kc), "s"(vc), "s"(wq), "s"(wo), "s"(ws));
  const int tid = threadIdx.x, lane = tid & 63;
  const int w = __builtin_amdgcn_readfirstlane(tid >> 6);
  const int gq = lane >> 4, c = lane & 15;
  const int bid = blockIdx.x;
  const int g = bid & 7, r = bid >> 3, b = r >> 3, j = r & 7;
  const int og = b * 8 + g;
  const bool is_o = g & 1;
  const int bp = r & 1, nh = (r >> 1) & 7, xh = g >> 1;
  const bool is_f = !is_o && r < 28;
  const int cg = xh * 14 + (r >> 1);
  const int go = (g + 1 + (r >> 4)) & 7;
  const rsrc_t rs = mkrs(ws, WS_END);
  const size_t kvbase = ((size_t)og * 4096 + j * 512) * 256;

  if (w < 4 && !is_o) {
    __builtin_amdgcn_s_setprio(3);
    const int kq = r & 3, qh = (r >> 2) & 3;
    v4u xr;
    unsigned ep;
    asm volatile("global_load_dword %0, %2, %3 sc1\n\tglobal_load_dwordx4 %1, %4, %5"
                 : "=&v"(ep), "=&v"(xr) : "v"(0u), "s"(ws + WS_EPOCH), "v"(lane * 16), "s"(xp + w * 1024) : "memory");
#pragma unroll
    for (int i = 0; i < 8; ++i)
      dma_nt(wq + go * 1024 + qh * 256 + (size_t)(kq * 128 + w * 32 + i * 4) * 8192,
             gq * 8192 + ((c ^ (gq << 1) ^ ((i & 2) << 2)) << 4), lds0 + L_DN + (w * 8 + i) * 1024);
    asm volatile("s_waitcnt vmcnt(8)" : "+v"(xr), "+v"(ep) :: "memory");
    const unsigned t32 = ep + 0x11111111u;
    lds<v4u>(lds0 + L_XS + 4096 + w * 1024 + lane * 16) = xr;
    float xf[8];
#pragma unroll
    for (int d = 0; d < 4; ++d) { xf[2 * d] = bflo(xr[d]); xf[2 * d + 1] = bfhi(xr[d]); }
    const float mean = wave_sum(((xf[0] + xf[1]) + (xf[2] + xf[3])) + ((xf[4] + xf[5]) + (xf[6] + xf[7]))) * (1.f / 512.f);
    float vs = 0.f;
#pragma unroll
    for (int i = 0; i < 8; ++i) { const float d = xf[i] - mean; vs = fmaf(d, d, vs); }
    vs = wave_sum(vs);
    const float mb = rb(mean);
    const float sd = rb(__builtin_amdgcn_sqrtf(rb(rb(vs * (1.f / 512.f)) + 1e-5f)));
    const float rsd = __builtin_amdgcn_rcpf(sd);
#pragma unroll
    for (int d = 0; d < 4; ++d) {
      const unsigned t = pk2(xf[2 * d] - mb, xf[2 * d + 1] - mb);
      xr[d] = pk2(bflo(t) * rsd, bfhi(t) * rsd);
    }
    lds<v4u>(lds0 + L_XS + w * 1024 + lane * 16) = xr;
    asm volatile("s_waitcnt vmcnt(0)" ::: "memory");
    lds_arrive(lds0 + F_XN);
    lds_wait(lds0 + F_XN, 4u);
    v4u xn[4];
#pragma unroll
    for (int i = 0; i < 4; ++i) xn[i] = lds<v4u>(lds0 + L_XS + (c & 3) * 1024 + kq * 256 + gq * 16 + i * 64);
#pragma unroll
    for (int tt = 0; tt < 2; ++tt) {
      v4f a = {0.f, 0.f, 0.f, 0.f};
#pragma unroll
      for (int i = 0; i < 4; ++i)
        a = mfma(xn[i], trfrag(lds0 + L_DN + (32 * i + 8 * gq + (c >> 2)) * 256 + ((2 * w + tt) ^ (c >> 2) ^ ((gq & 1) << 2)) * 32 + (c & 3) * 8, 1024), a);
      unsigned u = __float_as_uint(a.x);
      u = dppm<IDENT, 2, 0xf>(u, __float_as_uint(a.y));
      u = dppm<IDENT, 4, 0xf>(u, __float_as_uint(a.z));
      u = dppm<IDENT, 8, 0xf>(u, __float_as_uint(a.w));
      lds<volatile unsigned>(lds0 + L_XS + 8192 + w * 512 + gq * 128 + tt * 64 + c * 4) = (u & ~0xFu) | ((t32 >> (4 * (c & 7))) & 0xFu);
    }
    const v4u q4 = lds<volatile v4u>(lds0 + L_XS + 8192 + w * 512 + (lane & 31) * 16);
    __builtin_amdgcn_raw_buffer_store_b128(
        q4, rs, lane < 32 ? WS_QG + ((go * 4 + (lane >> 3)) * 4 + kq) * 2048 + (qh * 128 + w * 32) * 4 + (lane & 7) * 16 : OOB, 0, AUX_SC1);
    __builtin_amdgcn_s_setprio(0);
  }
  __builtin_amdgcn_sched_barrier(0);

  v4u kf[8], vr[8];
  const int ch0 = (w >> 2) * 32 + (w & 3) + bid;
  {
    const unsigned kv = (c >> 2) * 256 + (c & 3) * 16 + gq * 64;
#pragma unroll
    for (int i = 0; i < 8; ++i)
      asm volatile("global_load_dwordx4 %0, %1, %2 nt" : "=v"(kf[i]) : "v"(kv), "s"(kc + kvbase + ((ch0 + 4 * i) & 127) * 1024) : "memory");
  }
  lds_arrive(lds0 + F_KI);
  if (w >= 12) {
    __builtin_amdgcn_s_setprio(2);
    const unsigned qo = WS_QG + ((g * 4 + b) * 4 + (lane >> 5)) * 2048 + (w & 3) * 512 + (lane & 31) * 16;
    v4u q0, q1, p0, p1;
    unsigned t32;
    lds_wait(lds0 + F_KI, 16u);
    asm volatile("global_load_dword %2, %6, %5 sc1\n\tglobal_load_dwordx4 %0, %3, %5 sc1\n\tglobal_load_dwordx4 %1, %4, %5 sc1\n\ts_waitcnt vmcnt(0)"
                 : "=&v"(q0), "=&v"(q1), "=&v"(t32) : "v"(qo), "v"(qo + 4096), "s"(ws), "v"((unsigned)WS_EPOCH) : "memory");
    t32 += 0x11111111u;
    if (lane == 0) lds<unsigned>(lds0 + L_TAG) = t32;
    const unsigned tq = t32 >> (16 * (lane & 1));
    const v4u t4 = {tq & 0xFu, (tq >> 4) & 0xFu, (tq >> 8) & 0xFu, (tq >> 12) & 0xFu};
#pragma nounroll
    for (int spin = 0; spin < SPIN_CAP; ++spin) {
      p0 = q0 ^ t4; p1 = q1 ^ t4;
      const u64 m = __builtin_amdgcn_ballot_w64(((or4(p0) | or4(p1)) & 0xFu) != 0);
      if (m == 0) break;
      asm volatile("s_wqm_b64 exec, %5\n\tglobal_load_dwordx4 %0, %2, %4 sc1\n\tglobal_load_dwordx4 %1, %3, %4 sc1\n\ts_mov_b64 exec, -1\n\ts_waitcnt vmcnt(0)"
                   : "+v"(q0), "+v"(q1) : "v"(qo), "v"(qo + 4096), "s"(ws), "s"(m) : "memory", "scc");
    }
    const v4f qs = asf(p0) + asf(p1);
    lds<unsigned>(lds0 + L_Q + (w & 3) * 256 + (lane & 31) * 8 + (lane >> 5) * 4) = pk2(swadd32(qs.x, qs.z), swadd32(qs.y, qs.w));
    lds_arrive(lds0 + F_Q);
  }
  v4q la;
  asm volatile("s_load_dwordx8 %0, %1, 0x30\n\ts_waitcnt lgkmcnt(0)" : "=&s"(la) : "s"(__builtin_amdgcn_kernarg_segment_ptr()) : "memory");
  const char* wg = (const char*)la.x;
  const char* wu = (const char*)la.y;
  const char* wd = (const char*)la.z;
  char* outp = (char*)la.w;

  const rsrc_t rv = mkrs(vc + kvbase, 131072);
  if (w < 12) {
    lds_wait(lds0 + F_KI, 16u);
#pragma unroll
    for (int i = 0; i < 8; ++i) vr[i] = __builtin_amdgcn_raw_buffer_load_b128(rv, lane * 16, ((ch0 + 4 * i) & 127) * 1024, AUX_NT);
    __builtin_amdgcn_sched_barrier(0);
    lds_arrive(lds0 + F_VI);
  } else {
#pragma unroll
    for (int i = 0; i < 8; ++i) asm volatile("" : "=v"(vr[i]));
  }
  asm volatile("s_waitcnt vmcnt(8)" : "+v"(kf[0]), "+v"(kf[1]), "+v"(kf[2]), "+v"(kf[3]), "+v"(kf[4]), "+v"(kf[5]), "+v"(kf[6]), "+v"(kf[7]) :: "memory");
  lds_wait(lds0 + F_Q, 4u);
  const unsigned tagf = __builtin_amdgcn_readfirstlane(lds<unsigned>(lds0 + L_TAG));
  const v4u tag = {tagf & 0xFFu, (tagf >> 8) & 0xFFu, (tagf >> 16) & 0xFFu, tagf >> 24};

  v4u pm[4];
  {
    const v4u qf = lds<v4u>(lds0 + L_Q + (c >> 2) * 256 + (4 * gq + (c & 3)) * 16);
    float sc[8];
#pragma unroll
    for (int i = 0; i < 8; ++i) {
      const v4f a = mfma0(kf[i], qf);
      sc[i] = (dppf<0x00>(a.x) + dppf<0x55>(a.y)) + (dppf<0xAA>(a.z) + dppf<0xFF>(a.w));
    }
#pragma unroll
    for (int d = 0; d < 4; ++d) {
      const unsigned u1 = pk2(sc[2 * d], sc[2 * d + 1]);
      const unsigned u2 = pk2(bflo(u1) * 0.08838834764831845f, bfhi(u1) * 0.08838834764831845f);
      sc[2 * d] = bflo(u2); sc[2 * d + 1] = bfhi(u2);
    }
    float mw = fmaxf(fmaxf(fmaxf(sc[0], sc[1]), fmaxf(sc[2], sc[3])), fmaxf(fmaxf(sc[4], sc[5]), fmaxf(sc[6], sc[7])));
    mw = xrow_max(mw);
    float lw = 0.f;
#pragma unroll
    for (int i = 0; i < 8; ++i) { sc[i] = __builtin_amdgcn_exp2f((sc[i] - mw) * LOG2E); lw += sc[i]; }
    lw = xrow_sum(lw);
    if (gq == 0 && (c & 3) == 0) lds<v2f>(lds0 + L_SM + ((c >> 2) * 16 + w) * 8) = v2f{mw, lw};
    lds_arrive(lds0 + F_ST);
    v4u pb;
#pragma unroll
    for (int t = 0; t < 4; ++t) pb[t] = pk2(sc[2 * t], sc[2 * t + 1]);
#pragma unroll
    for (int e4 = 0; e4 < 4; ++e4) {
      const bool on = (c & 3) == e4;
      pm[e4] = v4u{on ? pb.x : 0u, on ? pb.y : 0u, on ? pb.z : 0u, on ? pb.w : 0u};
      PIN(pm[e4]);
    }
  }

  const int u8 = (w & 3) + 4 * (w >> 3);
  const bool dataw = (w & 4) == 0;
  unsigned off = gq * 2048 + u8 * 256 + c * 16, rslot = lds0 + L_R + u8 * 1024 + lane * 16;
  unsigned tr0, tr1, tr2, tr3, rsrc, so, sslot;
  if (is_o) {
    const int swz = (c >> 3) | ((gq & 1) << 1);
    const unsigned tb = lds0 + L_A + u8 * 8192 + (8 * gq + (c >> 2)) * 128 + (c & 3) * 8;
    tr0 = tb + swz * 32; tr1 = tb + (swz ^ 1) * 32; tr2 = tb + (swz ^ 2) * 32; tr3 = tb + (swz ^ 3) * 32;
    rsrc = lds0 + L_R + gq * 2048 + (w & 1) * 512 + c * 16;
    so = WS_HP + ((2 * bp + (w & 1)) * 8 + (r >> 4) * 4 + xh) * 2048 + nh * 256 + lane * 4;
    sslot = lds0 + L_FAC + (gq * 4 + (u8 >> 1)) * 4;
  } else {
    const unsigned tb = lds0 + L_A + u8 * 8192 + (4 * gq + (c >> 2)) * 64 + (c & 3) * 8;
    tr0 = tb + (gq & 1) * 32; tr1 = tb + ((gq & 1) ^ 1) * 32;
    tr2 = lds0 + L_DN + (16 * (gq & 1) + 8 * (gq >> 1) + (c >> 2)) * DN_PITCH + (gq & 1) * 128 + (w & 3) * 256 + (c & 3) * 8;
    tr3 = lds0 + L_S4 + (gq * 8 + (c & 7)) * 4;
    rsrc = lds0 + L_R + gq * 2048 + c * 16;
    so = WS_FP + ((2 * bp + (gq >> 1)) * 56 + cg) * 2048 + (w & 3) * 512 + (gq & 1) * 256 + c * 16;
    sslot = (lane == 0 || lane == 4 || lane == 36 || lane == 40) ? lds0 + L_S4 + (gq + ((lane >> 2) & 1)) * 32 + u8 * 4 : lds0 + L_DUMP + lane * 4;
  }
  const unsigned xrp = lds0 + L_XS + 4096 + (2 * bp + (c >> 3)) * 1024 + (64 * u8 + 4 * (c & 7) + gq) * 2;
  float xa = bflo(lds<unsigned short>(xrp)), xb = bflo(lds<unsigned short>(xrp + 64));
  PIN(xa); PIN(xb); PIN(off); PIN(tr0); PIN(tr1); PIN(tr2); PIN(tr3); PIN(rsrc); PIN(so); PIN(sslot); PIN(rslot);

  if (w >= 12) {
    lds_wait(lds0 + F_VI, 12u);
#pragma unroll
    for (int i = 0; i < 8; ++i) vr[i] = __builtin_amdgcn_raw_buffer_load_b128(rv, lane * 16, ((ch0 + 4 * i) & 127) * 1024, AUX_NT);
    __builtin_amdgcn_sched_barrier(0);
    lds_arrive(lds0 + F_VI);
  }

  {
    unsigned rp = lds0 + L_RED + w * RED_PITCH + (36 * (c >> 2) + 8 * gq + (c & 3)) * 16;
    unsigned fp = lds0 + (w < 12 ? F_PA : F_PB);
    PIN(rp); PIN(fp);
    v4u vf[8];
#pragma unroll
    for (int t = 0; t < 4; ++t) {
#pragma unroll
      for (int e = 0; e < 8; ++e)
        vf[e][t] = __builtin_amdgcn_perm(vr[2 * t + 1][e >> 1], vr[2 * t][e >> 1], (e & 1) ? 0x07060302u : 0x05040100u);
      if (t < 3) __builtin_amdgcn_sched_barrier(0);
    }
    v4f acc0 = {0.f, 0.f, 0.f, 0.f}, acc1 = acc0;
#pragma unroll
    for (int e4 = 0; e4 < 4; ++e4) {
      acc0 = mfma(vf[e4], pm[e4], acc0);
      acc1 = mfma(vf[4 + e4], pm[e4], acc1);
    }
    lds<v4f>(rp) = acc0;
    lds<v4f>(rp + 64) = acc1;
    lds_arrive(fp);
    __builtin_amdgcn_s_setprio(0);
  }

  if (w < 4) {
    const int hf = lane >> 5;
    __builtin_amdgcn_s_setprio(3);
    lds_wait(lds0 + F_ST, 16u);
    if (w == 0) {
      const v2f st = lds<v2f>(lds0 + L_SM + ((lane & 3) * 16 + (lane >> 2)) * 8);
      float M = st.x;
      M = fmaxf(M, dppf<ROR4>(M)); M = fmaxf(M, dppf<ROR8>(M)); M = xrow_max(M);
      float L = st.y * __builtin_amdgcn_exp2f((st.x - M) * LOG2E);
      L += dppf<ROR4>(L); L += dppf<ROR8>(L); L = xrow_sum(L);
      __builtin_amdgcn_raw_buffer_store_b128(v4u{__float_as_uint(M), tagf, __float_as_uint(L), tagf}, rs,
                                             lane < 4 ? WS_ST + (og * 8 + j) * 128 + lane * 16 : OOB, 0, AUX_SC1);
    }
    float M = lds<float>(lds0 + L_SM + (w * 16 + c) * 8);
    M = fmaxf(M, dppf<XOR1>(M)); M = fmaxf(M, dppf<XOR2>(M)); M = fmaxf(M, dppf<HMIR>(M)); M = fmaxf(M, dppf<ROR8>(M));
    float f[8];
#pragma unroll
    for (int i = 0; i < 8; ++i) {
      f[i] = __builtin_amdgcn_exp2f((lds<float>(lds0 + L_SM + (w * 16 + 2 * i + hf) * 8) - M) * LOG2E);
      PIN(f[i]);
    }
    const unsigned rp = lds0 + L_RED + hf * RED_PITCH + (36 * w + (lane & 31)) * 16;
    unsigned oo = WS_OP + (og * 8 + j) * 2048 + (32 * w + (lane & 31)) * 16 + hf * 8;
    unsigned tg = tagf >> (16 * hf);
    PIN(oo); PIN(tg);
    lds_wait(lds0 + F_PA, 12u);
    v4f s = {0.f, 0.f, 0.f, 0.f};
#pragma unroll
    for (int i = 0; i < 6; ++i) s += lds<v4f>(rp + 2 * i * RED_PITCH) * f[i];
    PIN(s);
    v4f t0, t1;
    __builtin_amdgcn_s_setprio(0);
#pragma nounroll
    for (int spin = 0; spin < LDS_CAP; ++spin) {
      const unsigned cnt = lds<volatile unsigned>(lds0 + F_PB);
      t0 = lds<volatile v4f>(rp + 12 * RED_PITCH);
      t1 = lds<volatile v4f>(rp + 14 * RED_PITCH);
      if (__builtin_amdgcn_readfirstlane(cnt) == 4u) break;
    }
    s += t0 * f[6];
    s += t1 * f[7];
    __builtin_amdgcn_raw_buffer_store_b64(v2u{tagw<0>(swadd32(s.x, s.z), tg), tagw<1>(swadd32(s.y, s.w), tg)}, rs, oo, 0, AUX_SC1);
    lds_arrive(lds0 + F_PUB);
  } else if (w < 8) {
    const int u = w & 3;
    lds_wait(lds0 + F_VI, 16u);
    if (is_o) {
      const char* src = wo + (size_t)(go * 512 + u * 8) * 1024 + nh * 128;
      const unsigned sv = (lane >> 3) * 1024 + (((lane & 7) ^ (((lane >> 4) & 1) << 1) ^ ((u & 1) << 2)) << 4);
#pragma unroll
      for (int i = 0; i < 16; ++i) dma_nt(src + i * 32768, sv, lds0 + L_A + u * 1024 + i * 4096);
      lds_arrive(lds0 + F_WI);
    } else {
      const unsigned pv = min(lane, 55) * 16;
#pragma nounroll
      for (int k = 0; k < 8; ++k) {
        const char* p = (k < 4 ? wg : wu) + (size_t)(16 * r + 4 * u + (k & 3)) * 3584 + xh * 896;
        dma_nt(p, pv, lds0 + L_DUMP);
        dma_l2(p, pv, lds0 + L_DUMP);
      }
      lds_wait(lds0 + F_PUB, 4u);
      if (is_f) {
#pragma unroll
        for (int i = 0; i < 8; ++i) dma_nt(wd + cg * 32768 + (8 * u + i) * 1024, lane * 16, lds0 + L_DN + (8 * u + i) * DN_PITCH + (u >> 1) * 128);
        const unsigned gv = (4 * (c >> 2) + gq) * 3584 + (((lane & 3) ^ ((gq & 1) << 1)) << 4);
#pragma unroll
        for (int i = 0; i < 16; ++i) {
          const int kb = 4 * u + ((i >> 1) & 3);
          if (i == 8) lds_arrive(lds0 + F_WI);
          dma_l2((i < 8 ? wg : wu) + (size_t)(32 * kb + 16 * (i & 1)) * 3584 + cg * 64, gv, lds0 + L_A + kb * 4096 + (i >> 3) * 2048 + (i & 1) * 1024);
        }
      } else {
        lds_arrive(lds0 + F_WI);
      }
    }
    asm volatile("s_waitcnt vmcnt(0)" ::: "memory");
    lds_arrive(lds0 + F_WO);
  } else if (w == 9) {
    const char* pc;
    int e0, e1;
    asm volatile("s_getpc_b64 %0\n.Lfb_here:\n\ts_mov_b32 %1, .Lfb_end_o-.Lfb_here\n\ts_mov_b32 %2, .Lfb_end_f-.Lfb_here" : "=s"(pc), "=s"(e0), "=s"(e1));
    dma_l2((const char*)((size_t)pc & ~(size_t)15), min((r & 15) * 1024 + lane * 16, max(max(e0, e1) - 16, 0) & ~15), lds0 + L_DUMP);
  } else if (w == 13 && is_o) {
    __builtin_amdgcn_s_setprio(3);
    v4u st;
#pragma nounroll
    for (int spin = 0; spin < SPIN_CAP; ++spin) {
      asm volatile("" ::: "memory");
      st = __builtin_amdgcn_raw_buffer_load_b128(rs, WS_ST + (((2 * bp + (lane >> 5)) * 8 + go) * 8 + ((lane >> 2) & 7)) * 128 + (lane & 3) * 16, 0, AUX_SC1);
      if (__builtin_amdgcn_ballot_w64(((st.y ^ tagf) | (st.w ^ tagf)) != 0) == 0) break;
      __builtin_amdgcn_s_sleep(2);
    }
    const float m = __uint_as_float(st.x);
    float M = fmaxf(m, dppf<ROR4>(m)); M = fmaxf(M, dppf<ROR8>(M));
    auto ms = __builtin_amdgcn_permlane16_swap(__float_as_uint(M), __float_as_uint(M), false, false);
    M = fmaxf(__uint_as_float(ms[0]), __uint_as_float(ms[1]));
    const float wt = __builtin_amdgcn_exp2f((m - M) * LOG2E);
    float L = __uint_as_float(st.z) * wt;
    L += dppf<ROR4>(L); L += dppf<ROR8>(L); L = swadd16(L, L);
    lds<float>(lds0 + L_FAC + lane * 4) = wt * __builtin_amdgcn_rcpf(L);
    lds_arrive(lds0 + F_WO);
    __builtin_amdgcn_s_setprio(0);
  }

  if (is_o) {
    if (dataw) {
      const char* pa = ws + WS_OP + (2 * bp * 8 + go) * 16384;
      Src p = {pa, pa + 131072, pa + 8192, pa + 131072 + 8192};
      unsigned fa = lds0 + F_A;
      asm volatile("" : "+s"(p.a), "+s"(p.b), "+s"(p.c), "+s"(p.d), "+v"(fa));
      Quad q, e;
      asm volatile("" : "=v"(q.a), "=v"(q.b), "=v"(q.c), "=v"(q.d));
      lds_wait(lds0 + F_WI, 4u);
      quad_issue(q, off, p, ~0ull);
      lds_wait<false>(lds0 + F_WO, 5u);
      const float f0 = lds<float>(sslot), f1 = lds<float>(sslot + 128), f2 = lds<float>(sslot + 64), f3 = lds<float>(sslot + 192);
      v4u wf[8];
#pragma unroll
      for (int t = 0; t < 8; ++t) wf[t] = trfrag(((t & 3) == 0 ? tr0 : (t & 3) == 1 ? tr1 : (t & 3) == 2 ? tr2 : tr3) + (t >> 2) * 4096, 512);
      quad_wait(q);
      quad_finish(q, e, tag, off, p, ~0ull);
      lds_arrive(fa);
      __builtin_amdgcn_sched_barrier(0);
      unsigned z0, z1;
      rows_scatter2(asf(e.a) * f0 + asf(e.c) * f2, asf(e.b) * f1 + asf(e.d) * f3, z0, z1);
      const v4u oa = pack8(__uint_as_float(dppm<ROR8, 0xf, 0xc>(z0, z1))), ob = pack8(__uint_as_float(dppm<ROR8, 0xf, 0x3>(z1, z0)));
      v4f m[4];
#pragma unroll
      for (int t = 0; t < 4; ++t) m[t] = mfma0(oa, wf[t]);
#pragma unroll
      for (int t = 0; t < 4; ++t) m[t] = mfma(ob, wf[4 + t], m[t]);
      asm volatile("" : "+v"(m[0]), "+v"(m[1]), "+v"(m[2]), "+v"(m[3]));
      lds<v4f>(rslot) = v4f{m[0].x, m[1].x, m[2].x, m[3].x};
    } else if (w == 12) {
      unsigned fo = lane < 57 ? WS_FB + ((bp * 57 + lane) * 16 + nh * 2 + (r >> 4)) * 128 + xh * 4 : OOB;
      PIN(fo);
      lds_wait<false>(lds0 + F_A, 8u);
      __builtin_amdgcn_raw_buffer_store_b32(tagf, rs, fo, 0, AUX_SC1);
    }
    if ((w >> 1) != 2) {
      bar();
      const char* fp = ws + WS_FC + ((xh * 32 + r) * 14 + (w < 4 ? w : w - 2)) * 128;
#pragma nounroll
      for (int spin = 0; spin < HINT_CAP; ++spin) {
        v4u f;
        asm volatile("s_load_dwordx4 %0, %1, 0x0 glc\n\ts_waitcnt lgkmcnt(0)" : "=&s"(f) : "s"(fp) : "memory");
        if (((f.x ^ tagf) | (f.y ^ tagf) | (f.z ^ tagf) | (f.w ^ tagf)) == 0) break;
      }
      lds_arrive(lds0 + F_C);
    } else {
      unsigned tg = tagf >> (8 * (lane & 3));
      PIN(tg);
      bar();
      __builtin_amdgcn_raw_buffer_store_b32(tagw<0>(rows_scatter(sum2(rsrc)), tg), rs, so, 0, AUX_SC1);
      if (w == 4) {
        const int fcol = 64 * (r >> 2) + 16 * gq + 4 * (r & 3) + (c & 3);
        const rsrc_t ro = mkrs(outp, 4096);
        unsigned oo = c < 4 ? xh * 1024 + fcol * 2 : OOB;
        unsigned fo = (lane >> 2) * 2048 + r * 64 + (lane & 3) * 16;
        const char* pf = ws + WS_FP + xh * 56 * 2048;
        Src p = {pf, pf + 16 * 2048, pf + 32 * 2048, pf + 44 * 2048};
        asm volatile("" : "+s"(p.a), "+s"(p.b), "+s"(p.c), "+s"(p.d));
        Quad q, e;
        asm volatile("" : "=v"(q.a), "=v"(q.b));
        q.c = tag; q.d = tag;
        PIN(oo); PIN(fo); PIN(q.c); PIN(q.d);
        v2u h0;
#pragma nounroll
        for (int spin = 0; spin < SPIN_CAP; ++spin) {
          asm volatile("" ::: "memory");
          h0 = __builtin_amdgcn_raw_buffer_load_b64(rs, WS_HR + (xh * 512 + fcol) * 8, 0, AUX_SC1);
          if (__builtin_amdgcn_ballot_w64((((h0.x ^ tagf) & 0xFFFFu) | (h0.y ^ tagf)) != 0) == 0) break;
        }
        float hv = bfhi(h0.x);
        PIN(hv);
        lds_wait<false>(lds0 + F_C, 14u);
        quad_issue(q, fo, p, 0xFFFFFFFFFFFFull);
        quad_wait(q);
        quad_finish(q, e, tag, fo, p, 0xFFFFFFFFFFFFull);
        float s = rows_scatter((asf(e.a) + asf(e.b)) + (asf(e.c) + asf(e.d)));
        s += dppf<ROR4>(s); s += dppf<ROR8>(s);
        const float o = hv + rb(s);
        __builtin_amdgcn_raw_buffer_store_b16((unsigned short)pk2(o, o), ro, oo, 0, 0);
        asm volatile(".Lfb_end_o:" ::: "memory");
      } else if (bid == 1) {
#pragma nounroll
        for (int spin = 0; spin < SPIN_CAP; ++spin) {
          asm volatile("" ::: "memory");
          const v2u h0 = __builtin_amdgcn_raw_buffer_load_b64(rs, WS_HR + (lane & 3) * 4096 + (lane & 60) * 8, 0, AUX_SC1);
          if (__builtin_amdgcn_ballot_w64(h0.y != tagf) == 0) break;
          __builtin_amdgcn_s_sleep(8);
        }
        if (lane == 0) __hip_atomic_store((GAS unsigned*)(ws + WS_EPOCH), tagf, __ATOMIC_RELAXED, __HIP_MEMORY_SCOPE_AGENT);
      }
    }
  } else if (is_f || cg == 14) {
    if (dataw) {
      const char* pa = ws + WS_HP + 2 * bp * 16384;
      const Src p = {pa, pa + 16384, pa + 8192, pa + 16384 + 8192};
      const char* fp = ws + WS_FB + ((bp * 57 + (is_f ? cg : 56)) * 16 + 2 * u8) * 128;
      unsigned fc = lds0 + F_C;
      v4u ha = {ONES, ONES, ONES, ONES}, hb = ha;
      PIN(ha); PIN(hb); PIN(fc);
      Quad q, e;
      asm volatile("" : "=v"(q.a), "=v"(q.b), "=v"(q.c), "=v"(q.d));
      lds_wait(lds0 + F_WI, 4u);
      quad_hinted(q, off, p, fp, ((u64)tagf << 32) | tagf);
      lds_wait<false>(lds0 + F_WO, 4u);
      v4u gf[8];
#pragma unroll
      for (int t = 0; t < 8; ++t) gf[t] = trfrag(((t & 1) ? tr1 : tr0) + ((t >> 1) & 1) * 2048 + (t >> 2) * 4096, 1024);
      quad_wait(q);
      lds_arrive(fc);
      quad_finish(q, e, tag, off, p, ~0ull);
      unsigned z0, z1;
      rows_scatter2(asf(e.a) + asf(e.c), asf(e.b) + asf(e.d), z0, z1);
      const unsigned zz = pk2(__uint_as_float(dppm<ROR8, 0xf, 0xc>(z0, z1)), __uint_as_float(dppm<ROR8, 0xf, 0x3>(z1, z0)));
      const float za = xa + bflo(zz), zb = xb + bfhi(zz);
      const unsigned ra = pk2(za, dppf<SHL1>(za)), rb2 = pk2(zb, dppf<SHL1>(zb));
      if (__builtin_expect(is_f, 1)) {
        ha = v4u{dppm<IDENT, 0xf, 0xd>(ha.x, ra), dppm<SHL2, 0xf, 0xd>(ha.y, ra), dppm<SHL4, 0xf, 0xd>(ha.z, ra), dppm<SHL6, 0xf, 0xd>(ha.w, ra)};
        hb = v4u{dppm<IDENT, 0xf, 0xd>(hb.x, rb2), dppm<SHL2, 0xf, 0xd>(hb.y, rb2), dppm<SHL4, 0xf, 0xd>(hb.z, rb2), dppm<SHL6, 0xf, 0xd>(hb.w, rb2)};
        v4f m[5];
#pragma unroll
        for (int t = 0; t < 5; ++t) m[t] = mfma0(ha, t < 4 ? gf[t] : ha);
        m[4] = mfma(hb, hb, m[4]);
#pragma unroll
        for (int t = 0; t < 4; ++t) m[t] = mfma(hb, gf[4 + t], m[t]);
        asm volatile("" : "+v"(m[0]), "+v"(m[1]), "+v"(m[2]), "+v"(m[3]), "+v"(m[4]));
        lds<float>(sslot) = m[4].x;
        lds<v4f>(rslot) = v4f{m[0].x, m[1].x, m[2].x, m[3].x};
        bar();
      } else {
        const int ho = WS_HR + ((2 * bp + (c >> 3)) * 512 + 64 * u8 + 4 * (c & 7) + gq) * 8;
        __builtin_amdgcn_raw_buffer_store_b64(v2u{(ra << 16) | (tagf & 0xFFFFu), tagf}, rs, ho, 0, AUX_SC1);
        __builtin_amdgcn_raw_buffer_store_b64(v2u{(rb2 << 16) | (tagf & 0xFFFFu), tagf}, rs, ho + 256, 0, AUX_SC1);
      }
    } else if (is_f && w == 4) {
      unsigned fo = WS_FC + ((64 * bp + lane) * 14 + (r >> 1)) * 128 + xh * 4;
      PIN(fo);
      lds_wait<false>(lds0 + F_C, 8u);
      __builtin_amdgcn_s_sleep(C_DELAY);
      __builtin_amdgcn_raw_buffer_store_b32(tagf, rs, fo, 0, AUX_SC1);
      bar();
    } else if (is_f && w >= 12) {
      v4u df[8], sel = {0x07060500u, 0x07060501u, 0x07060502u, 0x07060503u};
      lds_wait(lds0 + F_WO, 4u);
#pragma unroll
      for (int t = 0; t < 8; ++t) { df[t] = trfrag(tr2 + t * 32, 4 * DN_PITCH); PIN(df[t]); }
      PIN(sel);
      bar();
      float st = lds<float>(tr3);
      const v4f s0 = sum2(rsrc), s1 = sum2(rsrc + 512);
      const float cs = rows_scatter(sum2(rsrc + 256));
      auto cw = __builtin_amdgcn_permlane32_swap(__float_as_uint(cs), __float_as_uint(cs), false, false);
      st += dppf<XOR1>(st); st += dppf<XOR2>(st); st += dppf<HMIR>(st);
      auto sw = __builtin_amdgcn_permlane16_swap(__float_as_uint(st), __float_as_uint(st), false, false);
      const float mean = __uint_as_float(sw[1]) * (1.f / 512.f);
      const float var = fmaxf(__uint_as_float(sw[0]) * (1.f / 512.f) - mean * mean, 0.f);
      const float mb = rb(mean);
      const float sd = rb(__builtin_amdgcn_sqrtf(rb(rb(var) + 1e-5f)));
      const float rsd = __builtin_amdgcn_rcpf(sd);
      unsigned y0, y1;
      rows_scatter2(s0, s1, y0, y1);
      auto gu = __builtin_amdgcn_permlane32_swap(y0, y1, false, false);
      const unsigned y = pk2((__uint_as_float(gu[0]) - mb * __uint_as_float(cw[0])) * rsd, (__uint_as_float(gu[1]) - mb * __uint_as_float(cw[1])) * rsd);
      const float gv = bflo(y);
      const float sl = rb(gv * __builtin_amdgcn_rcpf(1.f + __builtin_amdgcn_exp2f(-gv * LOG2E)));
      const unsigned av = __float_as_uint(sl * bfhi(y));
      auto tr = __builtin_amdgcn_permlane32_swap(av, dppu<ROR8>(av), false, false);
      const float az = __uint_as_float(dppm<ROR8, 0xf, 0xc>(tr[0], tr[1]));
      const unsigned r1 = pk2(az, dppf<SHL1>(az));
      v4u af = {r1, dppu<SHL2>(r1), dppu<SHL4>(r1), dppu<SHL6>(r1)};
      af = v4u{dppm<SHR4, 0xf, 0xa>(af.x, r1), dppm<SHR2, 0xf, 0xa>(af.y, r1), dppm<IDENT, 0xf, 0xa>(af.z, r1), dppm<SHL2, 0xf, 0xa>(af.w, r1)};
      v4f m[8];
#pragma unroll
      for (int t = 0; t < 8; ++t) m[t] = mfma0(af, df[(t >> 1) + 4 * (t & 1)]);
      asm volatile("" : "+v"(m[0]), "+v"(m[1]), "+v"(m[2]), "+v"(m[3]));
      asm volatile("" : "+v"(m[4]), "+v"(m[5]), "+v"(m[6]), "+v"(m[7]));
      v4u y4;
#pragma unroll
      for (int t = 0; t < 4; ++t)
        y4[t] = __builtin_amdgcn_perm(dppm<IDENT, 0xa, 0xf>(__float_as_uint(m[2 * t].x), __float_as_uint(m[2 * t + 1].x)), tagf, sel[t]);
      __builtin_amdgcn_raw_buffer_store_b128(y4, rs, so, 0, AUX_SC1);
      asm volatile(".Lfb_end_f:" ::: "memory");
    } else if (is_f) {
      bar();
    }
  }
}

extern "C" void kernel_launch(void* const* d_in, const int* in_sizes, int n_in, void* d_out, int out_size, void* d_ws,
                              size_t ws_size, hipStream_t stream) {
  (void)hipFuncSetAttribute((const void*)fused_block, hipFuncAttributeMaxDynamicSharedMemorySize, LDS_BYTES);
  fused_block<<<256, 1024, LDS_BYTES, stream>>>((const char*)d_in[0], (const char*)d_in[1], (const char*)d_in[2], (const char*)d_in[3],
                                                (char*)d_ws, (const char*)d_in[4], (const char*)d_in[6], (const char*)d_in[7],
                                                (const char*)d_in[8], (char*)d_out);
}
```

```cpp
#include <hip/hip_runtime.h>
#include <stdint.h>

#define DIAG 0

#define GAS __attribute__((address_space(1)))
#define LAS __attribute__((address_space(3)))
typedef unsigned v4u __attribute__((ext_vector_type(4)));
typedef unsigned v2u __attribute__((ext_vector_type(2)));
typedef float v4f __attribute__((ext_vector_type(4)));
typedef float v2f __attribute__((ext_vector_type(2)));
typedef __bf16 v8bf __attribute__((ext_vector_type(8)));
typedef __bf16 v2bf __attribute__((ext_vector_type(2)));
typedef short v4s __attribute__((ext_vector_type(4)));
typedef unsigned long long u64;
typedef u64 v4q __attribute__((ext_vector_type(4)));
typedef __amdgpu_buffer_rsrc_t rsrc_t;

constexpr int AUX_NT = 2, AUX_SC1 = 16;
constexpr int SPIN_CAP = 1 << 20, LDS_CAP = 1 << 25, HINT_CAP = 1 << 10;
constexpr float LOG2E = 1.4426950408889634f;
constexpr unsigned ONES = 0x3F803F80u;
constexpr unsigned OOB = 0x80000000u;
#define C_DELAY 6

constexpr int WS_EPOCH = 0;
constexpr int WS_QG = 4096;
constexpr int WS_OP = WS_QG + 262144;
constexpr int WS_ST = WS_OP + 524288;
constexpr int WS_HP = WS_ST + 32768;
constexpr int WS_HR = WS_HP + 65536;
constexpr int WS_FP = WS_HR + 16384;
constexpr int WS_FB = WS_FP + 4 * 56 * 2048;
constexpr int WS_FC = WS_FB + 114 * 2048;
constexpr int WS_END = WS_FC + 128 * 14 * 128;

constexpr int L_A = 0;
constexpr int L_DN = 65536;
constexpr int DN_PITCH = 1056;
constexpr int L_RED = L_DN + 32 * DN_PITCH + 128;
constexpr int RED_PITCH = 2304;
constexpr int L_R = L_RED + 16 * RED_PITCH;
constexpr int L_XS = L_R;
constexpr int L_Q = L_R + 10240;
constexpr int L_SM = L_Q + 1024;
constexpr int L_S4 = L_SM + 512;
constexpr int L_FAC = L_S4 + 128;
constexpr int L_FLAG = L_FAC + 256;
constexpr int F_Q = L_FLAG, F_ST = L_FLAG + 4, F_PA = L_FLAG + 8, F_PB = L_FLAG + 12, F_A = L_FLAG + 16, F_C = L_FLAG + 20,
              F_XN = L_FLAG + 24, F_KI = L_FLAG + 28, F_VI = L_FLAG + 32, F_PUB = L_FLAG + 36, F_WI = L_FLAG + 40,
              F_WO = L_FLAG + 44, L_TAG = L_FLAG + 48;
constexpr int L_DUMP = L_FLAG + 64;
constexpr int L_TS = L_DUMP + 1024;
constexpr int LDS_BYTES = L_TS + (DIAG ? 4096 : 0);
static_assert(LDS_BYTES <= 163840 && LDS_BYTES > 81920, "one workgroup per CU");

__device__ __forceinline__ float bflo(unsigned u) { return __uint_as_float(u << 16); }
__device__ __forceinline__ float bfhi(unsigned u) { return __uint_as_float(u & 0xffff0000u); }
__device__ __forceinline__ unsigned pk2(float a, float b) {
  v2f f = {a, b};
  return __builtin_bit_cast(unsigned, __builtin_convertvector(f, v2bf));
}
__device__ __forceinline__ float rb(float a) { return bflo(pk2(a, a)); }
__device__ __forceinline__ v4f asf(v4u v) { return __builtin_bit_cast(v4f, v); }
template <int CTRL> __device__ __forceinline__ float dppf(float x) {
  return __int_as_float(__builtin_amdgcn_mov_dpp(__float_as_int(x), CTRL, 0xf, 0xf, true));
}
template <int CTRL> __device__ __forceinline__ unsigned dppu(unsigned x) {
  return (unsigned)__builtin_amdgcn_mov_dpp((int)x, CTRL, 0xf, 0xf, true);
}
template <int CTRL, int ROWM, int BANKM> __device__ __forceinline__ unsigned dppm(unsigned old, unsigned x) {
  return (unsigned)__builtin_amdgcn_update_dpp((int)old, (int)x, CTRL, ROWM, BANKM, false);
}
constexpr int XOR1 = 0xB1, XOR2 = 0x4E, HMIR = 0x141, ROR4 = 0x124, ROR8 = 0x128, IDENT = 0xE4;
constexpr int SHR2 = 0x112, SHR4 = 0x114, SHL1 = 0x101, SHL2 = 0x102, SHL4 = 0x104, SHL6 = 0x106;
__device__ __forceinline__ float swadd32(float a, float b) {
  auto s = __builtin_amdgcn_permlane32_swap(__float_as_uint(a), __float_as_uint(b), false, false);
  return __uint_as_float(s[0]) + __uint_as_float(s[1]);
}
__device__ __forceinline__ float swadd16(float a, float b) {
  auto s = __builtin_amdgcn_permlane16_swap(__float_as_uint(a), __float_as_uint(b), false, false);
  return __uint_as_float(s[0]) + __uint_as_float(s[1]);
}
__device__ __forceinline__ float xrow_sum(float x) { x = swadd16(x, x); return swadd32(x, x); }
__device__ __forceinline__ float wave_sum(float x) {
  x += dppf<XOR1>(x); x += dppf<XOR2>(x); x += dppf<HMIR>(x); x += dppf<ROR8>(x);
  return xrow_sum(x);
}
__device__ __forceinline__ float xrow_max(float x) {
  auto s = __builtin_amdgcn_permlane16_swap(__float_as_uint(x), __float_as_uint(x), false, false);
  x = fmaxf(__uint_as_float(s[0]), __uint_as_float(s[1]));
  auto t = __builtin_amdgcn_permlane32_swap(__float_as_uint(x), __float_as_uint(x), false, false);
  return fmaxf(__uint_as_float(t[0]), __uint_as_float(t[1]));
}
__device__ __forceinline__ float rows_scatter(v4f x) { return swadd16(swadd32(x.x, x.z), swadd32(x.y, x.w)); }
__device__ __forceinline__ void rows_scatter2(v4f x, v4f y, unsigned& zx, unsigned& zy) {
  float p = swadd32(x.x, x.z), q = swadd32(x.y, x.w), r = swadd32(y.x, y.z), s = swadd32(y.y, y.w);
  asm volatile("" : "+v"(p), "+v"(q), "+v"(r), "+v"(s));
  zx = __float_as_uint(swadd16(p, q)); zy = __float_as_uint(swadd16(r, s));
}
__device__ __forceinline__ v4u pack8(float z) {
  const unsigned r0 = pk2(z, dppf<SHL1>(z));
  return v4u{r0, dppu<SHL2>(r0), dppu<SHL4>(r0), dppu<SHL6>(r0)};
}
__device__ __forceinline__ v4f mfma(v4u a, v4u b, v4f c) {
  return __builtin_amdgcn_mfma_f32_16x16x32_bf16(__builtin_bit_cast(v8bf, a), __builtin_bit_cast(v8bf, b), c, 0, 0, 0);
}
__device__ __forceinline__ v4f mfma0(v4u a, v4u b) { return mfma(a, b, v4f{0.f, 0.f, 0.f, 0.f}); }
__device__ __forceinline__ rsrc_t mkrs(const void* p, unsigned bytes) {
  return __builtin_amdgcn_make_buffer_rsrc((void*)p, 0, bytes, 0x00020000);
}
#define PIN(x) asm volatile("" : "+v"(x))

__device__ __forceinline__ void bar() { asm volatile("s_waitcnt lgkmcnt(0)\n\ts_barrier" ::: "memory"); }
template <bool SLEEP = true> __device__ __forceinline__ void lds_wait(unsigned p, unsigned want) {
#pragma nounroll
  for (int spin = 0; spin < LDS_CAP; ++spin) {
    if ((unsigned)__builtin_amdgcn_readfirstlane(*(volatile LAS unsigned*)(size_t)p) >= want) break;
    if (SLEEP) __builtin_amdgcn_s_sleep(1);
  }
  asm volatile("" ::: "memory");
}
__device__ __forceinline__ void lds_arrive(unsigned p) {
  asm volatile("s_mov_b64 exec, 1\n\tds_add_u32 %0, %1\n\ts_mov_b64 exec, -1" :: "v"(p), "v"(1u) : "memory");
}

__device__ __forceinline__ void dma_nt(const char* sbase, unsigned voff, unsigned lds_dst) {
  unsigned keep;
  asm volatile("s_mov_b32 %0, m0\n\ts_mov_b32 m0, %3\n\ts_nop 0\n\tglobal_load_lds_dwordx4 %1, %2 nt\n\ts_mov_b32 m0, %0"
               : "=&s"(keep) : "v"(voff), "s"(sbase), "s"(lds_dst) : "memory");
}
__device__ __forceinline__ void dma_l2(const char* sbase, unsigned voff, unsigned lds_dst) {
  unsigned keep;
  asm volatile("s_mov_b32 %0, m0\n\ts_mov_b32 m0, %3\n\ts_nop 0\n\tglobal_load_lds_dwordx4 %1, %2 sc1\n\ts_mov_b32 m0, %0"
               : "=&s"(keep) : "v"(voff), "s"(sbase), "s"(lds_dst) : "memory");
}
__device__ __forceinline__ v4u trfrag(unsigned p, int second) {
  const v2u lo = __builtin_bit_cast(v2u, __builtin_amdgcn_ds_read_tr16_b64_v4i16((LAS v4s*)(size_t)p));
  const v2u hi = __builtin_bit_cast(v2u, __builtin_amdgcn_ds_read_tr16_b64_v4i16((LAS v4s*)(size_t)(p + second)));
  return v4u{lo.x, lo.y, hi.x, hi.y};
}
template <typename T> __device__ __forceinline__ LAS T& lds(unsigned p) { return *(LAS T*)(size_t)p; }
__device__ __forceinline__ v4f sum2(unsigned p) { return lds<v4f>(p) + lds<v4f>(p + 1024); }

template <int BYTE> __device__ __forceinline__ unsigned tagw(float f, unsigned tag) {
  return __builtin_amdgcn_perm(__float_as_uint(f), tag, 0x07060500u + BYTE);
}
__device__ __forceinline__ unsigned or4(v4u v) { return (v.x | v.y | v.z) | v.w; }
struct Quad { v4u a, b, c, d; };
struct Src { const char *a, *b, *c, *d; };
#define Q4OPS(q) "+v"(q.a), "+v"(q.b), "+v"(q.c), "+v"(q.d)
#define S4OPS(p) "s"(p.a), "s"(p.b), "s"(p.c), "s"(p.d)
#define Q4LOADS "s_wqm_b64 exec, %9\n\tglobal_load_dwordx4 %0, %4, %5 sc1\n\tglobal_load_dwordx4 %1, %4, %6 sc1\n\t" \
                "s_wqm_b64 exec, %10\n\tglobal_load_dwordx4 %2, %4, %7 sc1\n\tglobal_load_dwordx4 %3, %4, %8 sc1\n\ts_mov_b64 exec, -1"
__device__ __forceinline__ void quad_issue(Quad& q, unsigned off, const Src& p, u64 m1) {
  asm volatile(Q4LOADS : Q4OPS(q) : "v"(off), S4OPS(p), "s"(~0ull), "s"(m1) : "memory", "scc");
}
__device__ __forceinline__ void quad_hinted(Quad& q, unsigned off, const Src& p, const char* fp, u64 t64) {
  asm volatile(
      "s_mov_b64 s[88:89], -1\n\ts_mov_b64 s[90:91], -1\n\ts_movk_i32 s92, %11\n"
      "1:\n\ts_load_dwordx4 s[76:79], %9, 0x0 glc\n\ts_load_dwordx4 s[80:83], %9, 0x80 glc\n\ts_waitcnt lgkmcnt(0)\n\t"
      "s_xor_b64 s[76:77], s[76:77], %10\n\ts_xor_b64 s[78:79], s[78:79], %10\n\ts_xor_b64 s[80:81], s[80:81], %10\n\ts_xor_b64 s[82:83], s[82:83], %10\n\t"
      "s_or_b64 s[84:85], s[76:77], s[78:79]\n\ts_or_b64 s[86:87], s[80:81], s[82:83]\n\ts_or_b64 s[84:85], s[84:85], s[86:87]\n\t"
      "s_cbranch_scc0 2f\n\t"
      "s_cmp_eq_u32 s76, 0\n\ts_cselect_b32 s84, 0xffff, 0\n\ts_cmp_eq_u32 s77, 0\n\ts_cselect_b32 s76, 0xffff0000, 0\n\ts_or_b32 s84, s84, s76\n\t"
      "s_cmp_eq_u32 s78, 0\n\ts_cselect_b32 s85, 0xffff, 0\n\ts_cmp_eq_u32 s79, 0\n\ts_cselect_b32 s76, 0xffff0000, 0\n\ts_or_b32 s85, s85, s76\n\t"
      "s_cmp_eq_u32 s80, 0\n\ts_cselect_b32 s86, 0xffff, 0\n\ts_cmp_eq_u32 s81, 0\n\ts_cselect_b32 s76, 0xffff0000, 0\n\ts_or_b32 s86, s86, s76\n\t"
      "s_cmp_eq_u32 s82, 0\n\ts_cselect_b32 s87, 0xffff, 0\n\ts_cmp_eq_u32 s83, 0\n\ts_cselect_b32 s76, 0xffff0000, 0\n\ts_or_b32 s87, s87, s76\n\t"
      "s_and_b64 exec, s[84:85], s[88:89]\n\ts_cbranch_execz 3f\n\t"
      "global_load_dwordx4 %0, %4, %5 sc1\n\tglobal_load_dwordx4 %1, %4, %6 sc1\n\ts_andn2_b64 s[88:89], s[88:89], exec\n"
      "3:\n\ts_and_b64 exec, s[86:87], s[90:91]\n\ts_cbranch_execz 4f\n\t"
      "global_load_dwordx4 %2, %4, %7 sc1\n\tglobal_load_dwordx4 %3, %4, %8 sc1\n\ts_andn2_b64 s[90:91], s[90:91], exec\n"
      "4:\n\ts_sub_u32 s92, s92, 1\n\ts_cmp_lg_u32 s92, 0\n\ts_cbranch_scc1 1b\n"
      "2:\n\ts_mov_b64 exec, s[88:89]\n\tglobal_load_dwordx4 %0, %4, %5 sc1\n\tglobal_load_dwordx4 %1, %4, %6 sc1\n\t"
      "s_mov_b64 exec, s[90:91]\n\tglobal_load_dwordx4 %2, %4, %7 sc1\n\tglobal_load_dwordx4 %3, %4, %8 sc1\n\t"
      "s_mov_b64 exec, -1"
      : Q4OPS(q) : "v"(off), S4OPS(p), "s"(fp), "s"(t64), "n"(HINT_CAP)
      : "memory", "scc", "s76", "s77", "s78", "s79", "s80", "s81", "s82", "s83", "s84", "s85", "s86", "s87", "s88", "s89", "s90", "s91", "s92");
}
__device__ __forceinline__ void quad_wait(Quad& q) { asm volatile("s_waitcnt vmcnt(0)" : Q4OPS(q) :: "memory"); }
__device__ __forceinline__ u64 quad_strip(const Quad& q, Quad& e, v4u tag) {
  e.a = q.a ^ tag; e.b = q.b ^ tag; e.c = q.c ^ tag; e.d = q.d ^ tag;
  return __builtin_amdgcn_ballot_w64((((or4(e.a) | or4(e.b)) | (or4(e.c) | or4(e.d))) & 0xFFu) != 0);
}
__device__ __forceinline__ void quad_finish(Quad& q, Quad& e, v4u tag, unsigned off, const Src& p, u64 m1) {
  u64 m = quad_strip(q, e, tag);
  if (__builtin_expect(m != 0, 0)) {
#pragma nounroll
    for (int spin = 0; spin < SPIN_CAP && m != 0; ++spin) {
      asm volatile(Q4LOADS "\n\ts_waitcnt vmcnt(0)" : Q4OPS(q) : "v"(off), S4OPS(p), "s"(m), "s"(m & m1) : "memory", "scc");
      m = quad_strip(q, e, tag);
    }
  }
}

#define STAMP(i)
#define STAMPW(wv, i)

__global__ void __launch_bounds__(1024, 4)
fused_block(const char* __restrict__ xp, const char* __restrict__ kc, const char* __restrict__ vc, const char* __restrict__ wq,
            char* ws, const char* __restrict__ wo, const char* wg_, const char* wu_, const char* wd_, char* out_) {
  extern __shared__ __attribute__((aligned(16))) char smem_[];
  const unsigned lds0 = (unsigned)(size_t)(LAS char*)smem_;
  if (threadIdx.x < 12) lds<volatile unsigned>(lds0 + L_FLAG + threadIdx.x * 4) = 0u;
  bar();
  asm volatile("" :: "s"(xp), "s"(kc), "s"(vc), "s"(wq), "s"(wo), "s"(ws));
  const int tid = threadIdx.x, lane = tid & 63;
  const int w = __builtin_amdgcn_readfirstlane(tid >> 6);
  const int gq = lane >> 4, c = lane & 15;
  const int bid = blockIdx.x;
  const int g = bid & 7, r = bid >> 3, b = r >> 3, j = r & 7;
  const int og = b * 8 + g;
  const bool is_o = g & 1;
  const int bp = r & 1, nh = (r >> 1) & 7, xh = g >> 1;
  const bool is_f = !is_o && r < 28;
  const int cg = xh * 14 + (r >> 1);
  const int go = (g + 1 + (r >> 4)) & 7;
  const rsrc_t rs = mkrs(ws, WS_END);
  const size_t kvbase = ((size_t)og * 4096 + j * 512) * 256;
  STAMPW(0, 0);

  if (w < 4 && !is_o) {
    __builtin_amdgcn_s_setprio(3);
    const int kq = r & 3, qh = (r >> 2) & 3;
    v4u xr;
    unsigned ep;
    asm volatile("global_load_dword %0, %2, %3 sc1\n\tglobal_load_dwordx4 %1, %4, %5"
                 : "=&v"(ep), "=&v"(xr) : "v"(0u), "s"(ws + WS_EPOCH), "v"(lane * 16), "s"(xp + w * 1024) : "memory");
#pragma unroll
    for (int i = 0; i < 8; ++i)
      dma_nt(wq + go * 1024 + qh * 256 + (size_t)(kq * 128 + w * 32 + i * 4) * 8192,
             gq * 8192 + ((c ^ (gq << 1) ^ ((i & 2) << 2)) << 4), lds0 + L_DN + (w * 8 + i) * 1024);
    asm volatile("s_waitcnt vmcnt(8)" : "+v"(xr), "+v"(ep) :: "memory");
    const unsigned t32 = ep + 0x11111111u;
    lds<v4u>(lds0 + L_XS + 4096 + w * 1024 + lane * 16) = xr;
    float xf[8];
#pragma unroll
    for (int d = 0; d < 4; ++d) { xf[2 * d] = bflo(xr[d]); xf[2 * d + 1] = bfhi(xr[d]); }
    const float mean = wave_sum(((xf[0] + xf[1]) + (xf[2] + xf[3])) + ((xf[4] + xf[5]) + (xf[6] + xf[7]))) * (1.f / 512.f);
    float vs = 0.f;
#pragma unroll
    for (int i = 0; i < 8; ++i) { const float d = xf[i] - mean; vs = fmaf(d, d, vs); }
    vs = wave_sum(vs);
    const float mb = rb(mean);
    const float sd = rb(__builtin_amdgcn_sqrtf(rb(rb(vs * (1.f / 512.f)) + 1e-5f)));
    const float rsd = __builtin_amdgcn_rcpf(sd);
#pragma unroll
    for (int d = 0; d < 4; ++d) {
      const unsigned t = pk2(xf[2 * d] - mb, xf[2 * d + 1] - mb);
      xr[d] = pk2(bflo(t) * rsd, bfhi(t) * rsd);
    }
    lds<v4u>(lds0 + L_XS + w * 1024 + lane * 16) = xr;
    asm volatile("s_waitcnt vmcnt(0)" ::: "memory");
    lds_arrive(lds0 + F_XN);
    lds_wait(lds0 + F_XN, 4u);
    v4u xn[4];
#pragma unroll
    for (int i = 0; i < 4; ++i) xn[i] = lds<v4u>(lds0 + L_XS + (c & 3) * 1024 + kq * 256 + gq * 16 + i * 64);
#pragma unroll
    for (int tt = 0; tt < 2; ++tt) {
      v4f a = {0.f, 0.f, 0.f, 0.f};
#pragma unroll
      for (int i = 0; i < 4; ++i)
        a = mfma(xn[i], trfrag(lds0 + L_DN + (32 * i + 8 * gq + (c >> 2)) * 256 + ((2 * w + tt) ^ (c >> 2) ^ ((gq & 1) << 2)) * 32 + (c & 3) * 8, 1024), a);
      unsigned u = __float_as_uint(a.x);
      u = dppm<IDENT, 2, 0xf>(u, __float_as_uint(a.y));
      u = dppm<IDENT, 4, 0xf>(u, __float_as_uint(a.z));
      u = dppm<IDENT, 8, 0xf>(u, __float_as_uint(a.w));
      lds<volatile unsigned>(lds0 + L_XS + 8192 + w * 512 + gq * 128 + tt * 64 + c * 4) = (u & ~0xFu) | ((t32 >> (4 * (c & 7))) & 0xFu);
    }
    const v4u q4 = lds<volatile v4u>(lds0 + L_XS + 8192 + w * 512 + (lane & 31) * 16);
    __builtin_amdgcn_raw_buffer_store_b128(
        q4, rs, lane < 32 ? WS_QG + ((go * 4 + (lane >> 3)) * 4 + kq) * 2048 + (qh * 128 + w * 32) * 4 + (lane & 7) * 16 : OOB, 0, AUX_SC1);
    __builtin_amdgcn_s_setprio(0);
    STAMPW(0, 1);
  }
  __builtin_amdgcn_sched_barrier(0);

  v4u kf[8], vr[8];
  const int ch0 = (w >> 2) * 32 + (w & 3) + bid;
  {
    const unsigned kv = (c >> 2) * 256 + (c & 3) * 16 + gq * 64;
#pragma unroll
    for (int i = 0; i < 8; ++i)
      asm volatile("global_load_dwordx4 %0, %1, %2 nt" : "=v"(kf[i]) : "v"(kv), "s"(kc + kvbase + ((ch0 + 4 * i) & 127) * 1024) : "memory");
  }
  lds_arrive(lds0 + F_KI);
  if (w >= 12) {
    __builtin_amdgcn_s_setprio(2);
    const unsigned qo = WS_QG + ((g * 4 + b) * 4 + (lane >> 5)) * 2048 + (w & 3) * 512 + (lane & 31) * 16;
    v4u q0, q1, p0, p1;
    unsigned t32;
    lds_wait(lds0 + F_KI, 16u);
    asm volatile("global_load_dword %2, %6, %5 sc1\n\tglobal_load_dwordx4 %0, %3, %5 sc1\n\tglobal_load_dwordx4 %1, %4, %5 sc1\n\ts_waitcnt vmcnt(0)"
                 : "=&v"(q0), "=&v"(q1), "=&v"(t32) : "v"(qo), "v"(qo + 4096), "s"(ws), "v"((unsigned)WS_EPOCH) : "memory");
    t32 += 0x11111111u;
    if (lane == 0) lds<unsigned>(lds0 + L_TAG) = t32;
    const unsigned tq = t32 >> (16 * (lane & 1));
    const v4u t4 = {tq & 0xFu, (tq >> 4) & 0xFu, (tq >> 8) & 0xFu, (tq >> 12) & 0xFu};
#pragma nounroll
    for (int spin = 0; spin < SPIN_CAP; ++spin) {
      p0 = q0 ^ t4; p1 = q1 ^ t4;
      const u64 m = __builtin_amdgcn_ballot_w64(((or4(p0) | or4(p1)) & 0xFu) != 0);
      if (m == 0) break;
      asm volatile("s_wqm_b64 exec, %5\n\tglobal_load_dwordx4 %0, %2, %4 sc1\n\tglobal_load_dwordx4 %1, %3, %4 sc1\n\ts_mov_b64 exec, -1\n\ts_waitcnt vmcnt(0)"
                   : "+v"(q0), "+v"(q1) : "v"(qo), "v"(qo + 4096), "s"(ws), "s"(m) : "memory", "scc");
    }
    const v4f qs = asf(p0) + asf(p1);
    lds<unsigned>(lds0 + L_Q + (w & 3) * 256 + (lane & 31) * 8 + (lane >> 5) * 4) = pk2(swadd32(qs.x, qs.z), swadd32(qs.y, qs.w));
    lds_arrive(lds0 + F_Q);
    STAMPW(12, 2);
  }
  v4q la;
  asm volatile("s_load_dwordx8 %0, %1, 0x30\n\ts_waitcnt lgkmcnt(0)" : "=&s"(la) : "s"(__builtin_amdgcn_kernarg_segment_ptr()) : "memory");
  const char* wg = (const char*)la.x;
  const char* wu = (const char*)la.y;
  const char* wd = (const char*)la.z;
  char* outp = (char*)la.w;

  const rsrc_t rv = mkrs(vc + kvbase, 131072);
  if (w < 12) {
    lds_wait(lds0 + F_KI, 16u);
#pragma unroll
    for (int i = 0; i < 8; ++i) vr[i] = __builtin_amdgcn_raw_buffer_load_b128(rv, lane * 16, ((ch0 + 4 * i) & 127) * 1024, AUX_NT);
    __builtin_amdgcn_sched_barrier(0);
    lds_arrive(lds0 + F_VI);
  } else {
#pragma unroll
    for (int i = 0; i < 8; ++i) asm volatile("" : "=v"(vr[i]));
  }
  asm volatile("s_waitcnt vmcnt(8)" : "+v"(kf[0]), "+v"(kf[1]), "+v"(kf[2]), "+v"(kf[3]), "+v"(kf[4]), "+v"(kf[5]), "+v"(kf[6]), "+v"(kf[7]) :: "memory");
  lds_wait(lds0 + F_Q, 4u);
  const unsigned tagf = __builtin_amdgcn_readfirstlane(lds<unsigned>(lds0 + L_TAG));
  const v4u tag = {tagf & 0xFFu, (tagf >> 8) & 0xFFu, (tagf >> 16) & 0xFFu, tagf >> 24};

  v4u pm[4];
  {
    const v4u qf = lds<v4u>(lds0 + L_Q + (c >> 2) * 256 + (4 * gq + (c & 3)) * 16);
    float sc[8];
#pragma unroll
    for (int i = 0; i < 8; ++i) {
      const v4f a = mfma0(kf[i], qf);
      sc[i] = (dppf<0x00>(a.x) + dppf<0x55>(a.y)) + (dppf<0xAA>(a.z) + dppf<0xFF>(a.w));
    }
#pragma unroll
    for (int d = 0; d < 4; ++d) {
      const unsigned u1 = pk2(sc[2 * d], sc[2 * d + 1]);
      const unsigned u2 = pk2(bflo(u1) * 0.08838834764831845f, bfhi(u1) * 0.08838834764831845f);
      sc[2 * d] = bflo(u2); sc[2 * d + 1] = bfhi(u2);
    }
    float mw = fmaxf(fmaxf(fmaxf(sc[0], sc[1]), fmaxf(sc[2], sc[3])), fmaxf(fmaxf(sc[4], sc[5]), fmaxf(sc[6], sc[7])));
    mw = xrow_max(mw);
    float lw = 0.f;
#pragma unroll
    for (int i = 0; i < 8; ++i) { sc[i] = __builtin_amdgcn_exp2f((sc[i] - mw) * LOG2E); lw += sc[i]; }
    lw = xrow_sum(lw);
    if (gq == 0 && (c & 3) == 0) lds<v2f>(lds0 + L_SM + ((c >> 2) * 16 + w) * 8) = v2f{mw, lw};
    lds_arrive(lds0 + F_ST);
    v4u pb;
#pragma unroll
    for (int t = 0; t < 4; ++t) pb[t] = pk2(sc[2 * t], sc[2 * t + 1]);
#pragma unroll
    for (int e4 = 0; e4 < 4; ++e4) {
      const bool on = (c & 3) == e4;
      pm[e4] = v4u{on ? pb.x : 0u, on ? pb.y : 0u, on ? pb.z : 0u, on ? pb.w : 0u};
      PIN(pm[e4]);
    }
  }

  const int u8 = (w & 3) + 4 * (w >> 3);
  const bool dataw = (w & 4) == 0;
  unsigned off = gq * 2048 + u8 * 256 + c * 16, rslot = lds0 + L_R + u8 * 1024 + lane * 16;
  unsigned tr0, tr1, tr2, tr3, rsrc, so, sslot;
  if (is_o) {
    const int swz = (c >> 3) | ((gq & 1) << 1);
    const unsigned tb = lds0 + L_A + u8 * 8192 + (8 * gq + (c >> 2)) * 128 + (c & 3) * 8;
    tr0 = tb + swz * 32; tr1 = tb + (swz ^ 1) * 32; tr2 = tb + (swz ^ 2) * 32; tr3 = tb + (swz ^ 3) * 32;
    rsrc = lds0 + L_R + gq * 2048 + (w & 1) * 512 + c * 16;
    so = WS_HP + ((2 * bp + (w & 1)) * 8 + (r >> 4) * 4 + xh) * 2048 + nh * 256 + lane * 4;
    sslot = lds0 + L_FAC + (gq * 4 + (u8 >> 1)) * 4;
  } else {
    const unsigned tb = lds0 + L_A + u8 * 8192 + (4 * gq + (c >> 2)) * 64 + (c & 3) * 8;
    tr0 = tb + (gq & 1) * 32; tr1 = tb + ((gq & 1) ^ 1) * 32;
    tr2 = lds0 + L_DN + (16 * (gq & 1) + 8 * (gq >> 1) + (c >> 2)) * DN_PITCH + (gq & 1) * 128 + (w & 3) * 256 + (c & 3) * 8;
    tr3 = lds0 + L_S4 + (gq * 8 + (c & 7)) * 4;
    rsrc = lds0 + L_R + gq * 2048 + c * 16;
    so = WS_FP + ((2 * bp + (gq >> 1)) * 56 + cg) * 2048 + (w & 3) * 512 + (gq & 1) * 256 + c * 16;
    sslot = (lane == 0 || lane == 4 || lane == 36 || lane == 40) ? lds0 + L_S4 + (gq + ((lane >> 2) & 1)) * 32 + u8 * 4 : lds0 + L_DUMP + lane * 4;
  }
  const unsigned xrp = lds0 + L_XS + 4096 + (2 * bp + (c >> 3)) * 1024 + (64 * u8 + 4 * (c & 7) + gq) * 2;
  float xa = bflo(lds<unsigned short>(xrp)), xb = bflo(lds<unsigned short>(xrp + 64));
  PIN(xa); PIN(xb); PIN(off); PIN(tr0); PIN(tr1); PIN(tr2); PIN(tr3); PIN(rsrc); PIN(so); PIN(sslot); PIN(rslot);

  if (w >= 12) {
    lds_wait(lds0 + F_VI, 12u);
#pragma unroll
    for (int i = 0; i < 8; ++i) vr[i] = __builtin_amdgcn_raw_buffer_load_b128(rv, lane * 16, ((ch0 + 4 * i) & 127) * 1024, AUX_NT);
    __builtin_amdgcn_sched_barrier(0);
    lds_arrive(lds0 + F_VI);
  }
  STAMPW(15, 4);

  {
    unsigned rp = lds0 + L_RED + w * RED_PITCH + (36 * (c >> 2) + 8 * gq + (c & 3)) * 16;
    unsigned fp = lds0 + (w < 12 ? F_PA : F_PB);
    PIN(rp); PIN(fp);
    v4u vf[8];
#pragma unroll
    for (int t = 0; t < 4; ++t) {
#pragma unroll
      for (int e = 0; e < 8; ++e)
        vf[e][t] = __builtin_amdgcn_perm(vr[2 * t + 1][e >> 1], vr[2 * t][e >> 1], (e & 1) ? 0x07060302u : 0x05040100u);
      if (t < 3) __builtin_amdgcn_sched_barrier(0);
    }
    v4f acc0 = {0.f, 0.f, 0.f, 0.f}, acc1 = acc0;
#pragma unroll
    for (int e4 = 0; e4 < 4; ++e4) {
      acc0 = mfma(vf[e4], pm[e4], acc0);
      acc1 = mfma(vf[4 + e4], pm[e4], acc1);
    }
    lds<v4f>(rp) = acc0;
    lds<v4f>(rp + 64) = acc1;
    lds_arrive(fp);
    __builtin_amdgcn_s_setprio(0);
  }

  if (w < 4) {
    const int hf = lane >> 5;
    __builtin_amdgcn_s_setprio(3);
    lds_wait(lds0 + F_ST, 16u);
    if (w == 0) {
      const v2f st = lds<v2f>(lds0 + L_SM + ((lane & 3) * 16 + (lane >> 2)) * 8);
      float M = st.x;
      M = fmaxf(M, dppf<ROR4>(M)); M = fmaxf(M, dppf<ROR8>(M)); M = xrow_max(M);
      float L = st.y * __builtin_amdgcn_exp2f((st.x - M) * LOG2E);
      L += dppf<ROR4>(L); L += dppf<ROR8>(L); L = xrow_sum(L);
      __builtin_amdgcn_raw_buffer_store_b128(v4u{__float_as_uint(M), tagf, __float_as_uint(L), tagf}, rs,
                                             lane < 4 ? WS_ST + (og * 8 + j) * 128 + lane * 16 : OOB, 0, AUX_SC1);
    }
    float M = lds<float>(lds0 + L_SM + (w * 16 + c) * 8);
    M = fmaxf(M, dppf<XOR1>(M)); M = fmaxf(M, dppf<XOR2>(M)); M = fmaxf(M, dppf<HMIR>(M)); M = fmaxf(M, dppf<ROR8>(M));
    float f[8];
#pragma unroll
    for (int i = 0; i < 8; ++i) {
      f[i] = __builtin_amdgcn_exp2f((lds<float>(lds0 + L_SM + (w * 16 + 2 * i + hf) * 8) - M) * LOG2E);
      PIN(f[i]);
    }
    const unsigned rp = lds0 + L_RED + hf * RED_PITCH + (36 * w + (lane & 31)) * 16;
    unsigned oo = WS_OP + (og * 8 + j) * 2048 + (32 * w + (lane & 31)) * 16 + hf * 8;
    unsigned tg = tagf >> (16 * hf);
    PIN(oo); PIN(tg);
    lds_wait(lds0 + F_PA, 12u);
    v4f s = {0.f, 0.f, 0.f, 0.f};
#pragma unroll
    for (int i = 0; i < 6; ++i) s += lds<v4f>(rp + 2 * i * RED_PITCH) * f[i];
    PIN(s);
    v4f t0, t1;
    __builtin_amdgcn_s_setprio(0);
#pragma nounroll
    for (int spin = 0; spin < LDS_CAP; ++spin) {
      const unsigned cnt = lds<volatile unsigned>(lds0 + F_PB);
      t0 = lds<volatile v4f>(rp + 12 * RED_PITCH);
      t1 = lds<volatile v4f>(rp + 14 * RED_PITCH);
      if (__builtin_amdgcn_readfirstlane(cnt) == 4u) break;
    }
    s += t0 * f[6];
    s += t1 * f[7];
    __builtin_amdgcn_raw_buffer_store_b64(v2u{tagw<0>(swadd32(s.x, s.z), tg), tagw<1>(swadd32(s.y, s.w), tg)}, rs, oo, 0, AUX_SC1);
    lds_arrive(lds0 + F_PUB);
    STAMPW(0, 8);
  } else if (w < 8) {
    const int u = w & 3;
    lds_wait(lds0 + F_VI, 16u);
    if (is_o) {
      const char* src = wo + (size_t)(go * 512 + u * 8) * 1024 + nh * 128;
      const unsigned sv = (lane >> 3) * 1024 + (((lane & 7) ^ (((lane >> 4) & 1) << 1) ^ ((u & 1) << 2)) << 4);
#pragma unroll
      for (int i = 0; i < 16; ++i) dma_nt(src + i * 32768, sv, lds0 + L_A + u * 1024 + i * 4096);
      lds_arrive(lds0 + F_WI);
    } else {
      const unsigned pv = min(lane, 55) * 16;
#pragma nounroll
      for (int k = 0; k < 8; ++k) {
        const char* p = (k < 4 ? wg : wu) + (size_t)(16 * r + 4 * u + (k & 3)) * 3584 + xh * 896;
        dma_nt(p, pv, lds0 + L_DUMP);
        dma_l2(p, pv, lds0 + L_DUMP);
      }
      lds_wait(lds0 + F_PUB, 4u);
      if (is_f) {
#pragma unroll
        for (int i = 0; i < 8; ++i) dma_nt(wd + cg * 32768 + (8 * u + i) * 1024, lane * 16, lds0 + L_DN + (8 * u + i) * DN_PITCH + (u >> 1) * 128);
        const unsigned gv = (4 * (c >> 2) + gq) * 3584 + (((lane & 3) ^ ((gq & 1) << 1)) << 4);
#pragma unroll
        for (int i = 0; i < 16; ++i) {
          const int kb = 4 * u + ((i >> 1) & 3);
          if (i == 8) lds_arrive(lds0 + F_WI);
          dma_l2((i < 8 ? wg : wu) + (size_t)(32 * kb + 16 * (i & 1)) * 3584 + cg * 64, gv, lds0 + L_A + kb * 4096 + (i >> 3) * 2048 + (i & 1) * 1024);
        }
      } else {
        lds_arrive(lds0 + F_WI);
      }
    }
    asm volatile("s_waitcnt vmcnt(0)" ::: "memory");
    lds_arrive(lds0 + F_WO);
    STAMPW(4, 24);
  } else if (w == 9) {
    const char* pc;
    int e0, e1;
    asm volatile("s_getpc_b64 %0\n.Lfb_here:\n\ts_mov_b32 %1, .Lfb_end_o-.Lfb_here\n\ts_mov_b32 %2, .Lfb_end_f-.Lfb_here" : "=s"(pc), "=s"(e0), "=s"(e1));
    dma_l2((const char*)((size_t)pc & ~(size_t)15), min((r & 15) * 1024 + lane * 16, max(max(e0, e1) - 16, 0) & ~15), lds0 + L_DUMP);
  } else if (w == 13 && is_o) {
    __builtin_amdgcn_s_setprio(3);
    v4u st;
#pragma nounroll
    for (int spin = 0; spin < SPIN_CAP; ++spin) {
      asm volatile("" ::: "memory");
      st = __builtin_amdgcn_raw_buffer_load_b128(rs, WS_ST + (((2 * bp + (lane >> 5)) * 8 + go) * 8 + ((lane >> 2) & 7)) * 128 + (lane & 3) * 16, 0, AUX_SC1);
      if (__builtin_amdgcn_ballot_w64(((st.y ^ tagf) | (st.w ^ tagf)) != 0) == 0) break;
      __builtin_amdgcn_s_sleep(2);
    }
    const float m = __uint_as_float(st.x);
    float M = fmaxf(m, dppf<ROR4>(m)); M = fmaxf(M, dppf<ROR8>(M));
    auto ms = __builtin_amdgcn_permlane16_swap(__float_as_uint(M), __float_as_uint(M), false, false);
    M = fmaxf(__uint_as_float(ms[0]), __uint_as_float(ms[1]));
    const float wt = __builtin_amdgcn_exp2f((m - M) * LOG2E);
    float L = __uint_as_float(st.z) * wt;
    L += dppf<ROR4>(L); L += dppf<ROR8>(L); L = swadd16(L, L);
    lds<float>(lds0 + L_FAC + lane * 4) = wt * __builtin_amdgcn_rcpf(L);
    lds_arrive(lds0 + F_WO);
    __builtin_amdgcn_s_setprio(0);
  }

  if (is_o) {
    if (dataw) {
      const char* pa = ws + WS_OP + (2 * bp * 8 + go) * 16384;
      Src p = {pa, pa + 131072, pa + 8192, pa + 131072 + 8192};
      unsigned fa = lds0 + F_A;
      asm volatile("" : "+s"(p.a), "+s"(p.b), "+s"(p.c), "+s"(p.d), "+v"(fa));
      Quad q, e;
      asm volatile("" : "=v"(q.a), "=v"(q.b), "=v"(q.c), "=v"(q.d));
      lds_wait(lds0 + F_WI, 4u);
      quad_issue(q, off, p, ~0ull);
      lds_wait<false>(lds0 + F_WO, 5u);
      STAMPW(0, 19);
      const float f0 = lds<float>(sslot), f1 = lds<float>(sslot + 128), f2 = lds<float>(sslot + 64), f3 = lds<float>(sslot + 192);
      v4u wf[8];
#pragma unroll
      for (int t = 0; t < 8; ++t) wf[t] = trfrag(((t & 3) == 0 ? tr0 : (t & 3) == 1 ? tr1 : (t & 3) == 2 ? tr2 : tr3) + (t >> 2) * 4096, 512);
      quad_wait(q);
      quad_finish(q, e, tag, off, p, ~0ull);
      lds_arrive(fa);
      __builtin_amdgcn_sched_barrier(0);
      STAMPW(0, 10);
      unsigned z0, z1;
      rows_scatter2(asf(e.a) * f0 + asf(e.c) * f2, asf(e.b) * f1 + asf(e.d) * f3, z0, z1);
      const v4u oa = pack8(__uint_as_float(dppm<ROR8, 0xf, 0xc>(z0, z1))), ob = pack8(__uint_as_float(dppm<ROR8, 0xf, 0x3>(z1, z0)));
      v4f m[4];
#pragma unroll
      for (int t = 0; t < 4; ++t) m[t] = mfma0(oa, wf[t]);
#pragma unroll
      for (int t = 0; t < 4; ++t) m[t] = mfma(ob, wf[4 + t], m[t]);
      asm volatile("" : "+v"(m[0]), "+v"(m[1]), "+v"(m[2]), "+v"(m[3]));
      lds<v4f>(rslot) = v4f{m[0].x, m[1].x, m[2].x, m[3].x};
    } else if (w == 12) {
      unsigned fo = lane < 57 ? WS_FB + ((bp * 57 + lane) * 16 + nh * 2 + (r >> 4)) * 128 + xh * 4 : OOB;
      PIN(fo);
      lds_wait<false>(lds0 + F_A, 8u);
      __builtin_amdgcn_raw_buffer_store_b32(tagf, rs, fo, 0, AUX_SC1);
    }
    if ((w >> 1) != 2) {
      bar();
      const char* fp = ws + WS_FC + ((xh * 32 + r) * 14 + (w < 4 ? w : w - 2)) * 128;
#pragma nounroll
      for (int spin = 0; spin < HINT_CAP; ++spin) {
        v4u f;
        asm volatile("s_load_dwordx4 %0, %1, 0x0 glc\n\ts_waitcnt lgkmcnt(0)" : "=&s"(f) : "s"(fp) : "memory");
        if (((f.x ^ tagf) | (f.y ^ tagf) | (f.z ^ tagf) | (f.w ^ tagf)) == 0) break;
      }
      lds_arrive(lds0 + F_C);
    } else {
      unsigned tg = tagf >> (8 * (lane & 3));
      PIN(tg);
      bar();
      __builtin_amdgcn_raw_buffer_store_b32(tagw<0>(rows_scatter(sum2(rsrc)), tg), rs, so, 0, AUX_SC1);
      STAMPW(5, 11);
      if (w == 4) {
        const int fcol = 64 * (r >> 2) + 16 * gq + 4 * (r & 3) + (c & 3);
        const rsrc_t ro = mkrs(outp, 4096);
        unsigned oo = c < 4 ? xh * 1024 + fcol * 2 : OOB;
        unsigned fo = (lane >> 2) * 2048 + r * 64 + (lane & 3) * 16;
        const char* pf = ws + WS_FP + xh * 56 * 2048;
        Src p = {pf, pf + 16 * 2048, pf + 32 * 2048, pf + 44 * 2048};
        asm volatile("" : "+s"(p.a), "+s"(p.b), "+s"(p.c), "+s"(p.d));
        Quad q, e;
        asm volatile("" : "=v"(q.a), "=v"(q.b));
        q.c = tag; q.d = tag;
        PIN(oo); PIN(fo); PIN(q.c); PIN(q.d);
        v2u h0;
#pragma nounroll
        for (int spin = 0; spin < SPIN_CAP; ++spin) {
          asm volatile("" ::: "memory");
          h0 = __builtin_amdgcn_raw_buffer_load_b64(rs, WS_HR + (xh * 512 + fcol) * 8, 0, AUX_SC1);
          if (__builtin_amdgcn_ballot_w64((((h0.x ^ tagf) & 0xFFFFu) | (h0.y ^ tagf)) != 0) == 0) break;
        }
        float hv = bfhi(h0.x);
        PIN(hv);
        lds_wait<false>(lds0 + F_C, 14u);
        STAMP(25);
        quad_issue(q, fo, p, 0xFFFFFFFFFFFFull);
        quad_wait(q);
        quad_finish(q, e, tag, fo, p, 0xFFFFFFFFFFFFull);
        float s = rows_scatter((asf(e.a) + asf(e.b)) + (asf(e.c) + asf(e.d)));
        s += dppf<ROR4>(s); s += dppf<ROR8>(s);
        const float o = hv + rb(s);
        __builtin_amdgcn_raw_buffer_store_b16((unsigned short)pk2(o, o), ro, oo, 0, 0);
        asm volatile(".Lfb_end_o:" ::: "memory");
        STAMP(16);
      } else if (bid == 1) {
#pragma nounroll
        for (int spin = 0; spin < SPIN_CAP; ++spin) {
          asm volatile("" ::: "memory");
          const v2u h0 = __builtin_amdgcn_raw_buffer_load_b64(rs, WS_HR + (lane & 3) * 4096 + (lane & 60) * 8, 0, AUX_SC1);
          if (__builtin_amdgcn_ballot_w64(h0.y != tagf) == 0) break;
          __builtin_amdgcn_s_sleep(8);
        }
        if (lane == 0) __hip_atomic_store((GAS unsigned*)(ws + WS_EPOCH), tagf, __ATOMIC_RELAXED, __HIP_MEMORY_SCOPE_AGENT);
      }
    }
  } else if (is_f || cg == 14) {
    if (dataw) {
      const char* pa = ws + WS_HP + 2 * bp * 16384;
      const Src p = {pa, pa + 16384, pa + 8192, pa + 16384 + 8192};
      const char* fp = ws + WS_FB + ((bp * 57 + (is_f ? cg : 56)) * 16 + 2 * u8) * 128;
      unsigned fc = lds0 + F_C;
      v4u ha = {ONES, ONES, ONES, ONES}, hb = ha;
      PIN(ha); PIN(hb); PIN(fc);
      Quad q, e;
      asm volatile("" : "=v"(q.a), "=v"(q.b), "=v"(q.c), "=v"(q.d));
      lds_wait(lds0 + F_WI, 4u);
      quad_hinted(q, off, p, fp, ((u64)tagf << 32) | tagf);
      lds_wait<false>(lds0 + F_WO, 4u);
      v4u gf[8];
#pragma unroll
      for (int t = 0; t < 8; ++t) gf[t] = trfrag(((t & 1) ? tr1 : tr0) + ((t >> 1) & 1) * 2048 + (t >> 2) * 4096, 1024);
      quad_wait(q);
      lds_arrive(fc);
      STAMPW(0, 18);
      quad_finish(q, e, tag, off, p, ~0ull);
      unsigned z0, z1;
      rows_scatter2(asf(e.a) + asf(e.c), asf(e.b) + asf(e.d), z0, z1);
      const unsigned zz = pk2(__uint_as_float(dppm<ROR8, 0xf, 0xc>(z0, z1)), __uint_as_float(dppm<ROR8, 0xf, 0x3>(z1, z0)));
      const float za = xa + bflo(zz), zb = xb + bfhi(zz);
      const unsigned ra = pk2(za, dppf<SHL1>(za)), rb2 = pk2(zb, dppf<SHL1>(zb));
      if (__builtin_expect(is_f, 1)) {
        ha = v4u{dppm<IDENT, 0xf, 0xd>(ha.x, ra), dppm<SHL2, 0xf, 0xd>(ha.y, ra), dppm<SHL4, 0xf, 0xd>(ha.z, ra), dppm<SHL6, 0xf, 0xd>(ha.w, ra)};
        hb = v4u{dppm<IDENT, 0xf, 0xd>(hb.x, rb2), dppm<SHL2, 0xf, 0xd>(hb.y, rb2), dppm<SHL4, 0xf, 0xd>(hb.z, rb2), dppm<SHL6, 0xf, 0xd>(hb.w, rb2)};
        v4f m[5];
#pragma unroll
        for (int t = 0; t < 5; ++t) m[t] = mfma0(ha, t < 4 ? gf[t] : ha);
        m[4] = mfma(hb, hb, m[4]);
#pragma unroll
        for (int t = 0; t < 4; ++t) m[t] = mfma(hb, gf[4 + t], m[t]);
        asm volatile("" : "+v"(m[0]), "+v"(m[1]), "+v"(m[2]), "+v"(m[3]), "+v"(m[4]));
        lds<float>(sslot) = m[4].x;
        lds<v4f>(rslot) = v4f{m[0].x, m[1].x, m[2].x, m[3].x};
        bar();
      } else {
        const int ho = WS_HR + ((2 * bp + (c >> 3)) * 512 + 64 * u8 + 4 * (c & 7) + gq) * 8;
        __builtin_amdgcn_raw_buffer_store_b64(v2u{(ra << 16) | (tagf & 0xFFFFu), tagf}, rs, ho, 0, AUX_SC1);
        __builtin_amdgcn_raw_buffer_store_b64(v2u{(rb2 << 16) | (tagf & 0xFFFFu), tagf}, rs, ho + 256, 0, AUX_SC1);
      }
    } else if (is_f && w == 4) {
      unsigned fo = WS_FC + ((64 * bp + lane) * 14 + (r >> 1)) * 128 + xh * 4;
      PIN(fo);
      lds_wait<false>(lds0 + F_C, 8u);
      __builtin_amdgcn_s_sleep(C_DELAY);
      __builtin_amdgcn_raw_buffer_store_b32(tagf, rs, fo, 0, AUX_SC1);
      bar();
    } else if (is_f && w >= 12) {
      v4u df[8], sel = {0x07060500u, 0x07060501u, 0x07060502u, 0x07060503u};
      lds_wait(lds0 + F_WO, 4u);
#pragma unroll
      for (int t = 0; t < 8; ++t) { df[t] = trfrag(tr2 + t * 32, 4 * DN_PITCH); PIN(df[t]); }
      PIN(sel);
      bar();
      STAMPW(12, 13);
      float st = lds<float>(tr3);
      const v4f s0 = sum2(rsrc), s1 = sum2(rsrc + 512);
      const float cs = rows_scatter(sum2(rsrc + 256));
      auto cw = __builtin_amdgcn_permlane32_swap(__float_as_uint(cs), __float_as_uint(cs), false, false);
      st += dppf<XOR1>(st); st += dppf<XOR2>(st); st += dppf<HMIR>(st);
      auto sw = __builtin_amdgcn_permlane16_swap(__float_as_uint(st), __float_as_uint(st), false, false);
      const float mean = __uint_as_float(sw[1]) * (1.f / 512.f);
      const float var = fmaxf(__uint_as_float(sw[0]) * (1.f / 512.f) - mean * mean, 0.f);
      const float mb = rb(mean);
      const float sd = rb(__builtin_amdgcn_sqrtf(rb(rb(var) + 1e-5f)));
      const float rsd = __builtin_amdgcn_rcpf(sd);
      unsigned y0, y1;
      rows_scatter2(s0, s1, y0, y1);
      auto gu = __builtin_amdgcn_permlane32_swap(y0, y1, false, false);
      const unsigned y = pk2((__uint_as_float(gu[0]) - mb * __uint_as_float(cw[0])) * rsd, (__uint_as_float(gu[1]) - mb * __uint_as_float(cw[1])) * rsd);
      const float gv = bflo(y);
      const float sl = rb(gv * __builtin_amdgcn_rcpf(1.f + __builtin_amdgcn_exp2f(-gv * LOG2E)));
      const unsigned av = __float_as_uint(sl * bfhi(y));
      auto tr = __builtin_amdgcn_permlane32_swap(av, dppu<ROR8>(av), false, false);
      const float az = __uint_as_float(dppm<ROR8, 0xf, 0xc>(tr[0], tr[1]));
      const unsigned r1 = pk2(az, dppf<SHL1>(az));
      v4u af = {r1, dppu<SHL2>(r1), dppu<SHL4>(r1), dppu<SHL6>(r1)};
      af = v4u{dppm<SHR4, 0xf, 0xa>(af.x, r1), dppm<SHR2, 0xf, 0xa>(af.y, r1), dppm<IDENT, 0xf, 0xa>(af.z, r1), dppm<SHL2, 0xf, 0xa>(af.w, r1)};
      v4f m[8];
#pragma unroll
      for (int t = 0; t < 8; ++t) m[t] = mfma0(af, df[(t >> 1) + 4 * (t & 1)]);
      asm volatile("" : "+v"(m[0]), "+v"(m[1]), "+v"(m[2]), "+v"(m[3]));
      asm volatile("" : "+v"(m[4]), "+v"(m[5]), "+v"(m[6]), "+v"(m[7]));
      v4u y4;
#pragma unroll
      for (int t = 0; t < 4; ++t)
        y4[t] = __builtin_amdgcn_perm(dppm<IDENT, 0xa, 0xf>(__float_as_uint(m[2 * t].x), __float_as_uint(m[2 * t + 1].x)), tagf, sel[t]);
      __builtin_amdgcn_raw_buffer_store_b128(y4, rs, so, 0, AUX_SC1);
      asm volatile(".Lfb_end_f:" ::: "memory");
      STAMPW(12, 14);
    } else if (is_f) {
      bar();
    }
  }
}

extern "C" void kernel_launch(void* const* d_in, const int* in_sizes, int n_in, void* d_out, int out_size, void* d_ws,
                              size_t ws_size, hipStream_t stream) {
  (void)hipFuncSetAttribute((const void*)fused_block, hipFuncAttributeMaxDynamicSharedMemorySize, LDS_BYTES);
  fused_block<<<256, 1024, LDS_BYTES, stream>>>((const char*)d_in[0], (const char*)d_in[1], (const char*)d_in[2], (const char*)d_in[3],
                                                (char*)d_ws, (const char*)d_in[4], (const char*)d_in[6], (const char*)d_in[7],
                                                (const char*)d_in[8], (char*)d_out);
}
```

```cpp
#include <hip/hip_runtime.h>
#include <stdint.h>

#define GAS __attribute__((address_space(1)))
#define LAS __attribute__((address_space(3)))
typedef unsigned v4u __attribute__((ext_vector_type(4)));
typedef unsigned v2u __attribute__((ext_vector_type(2)));
typedef float v4f __attribute__((ext_vector_type(4)));
typedef float v2f __attribute__((ext_vector_type(2)));
typedef __bf16 v8bf __attribute__((ext_vector_type(8)));
typedef __bf16 v2bf __attribute__((ext_vector_type(2)));
typedef short v4s __attribute__((ext_vector_type(4)));
typedef unsigned long long u64;
typedef u64 v4q __attribute__((ext_vector_type(4)));
typedef __amdgpu_buffer_rsrc_t rsrc_t;

constexpr int AUX_NT = 2, AUX_SC1 = 16;
constexpr int SPIN_CAP = 1 << 20, LDS_CAP = 1 << 25, HINT_CAP = 1 << 10;
constexpr float LOG2E = 1.4426950408889634f;
constexpr unsigned ONES = 0x3F803F80u;
constexpr unsigned OOB = 0x80000000u;
constexpr int C_DELAY = 6;

constexpr int WS_EPOCH = 0;
constexpr int WS_QG = 4096;
constexpr int WS_OP = WS_QG + 262144;
constexpr int WS_ST = WS_OP + 524288;
constexpr int WS_HP = WS_ST + 32768;
constexpr int WS_HR = WS_HP + 65536;
constexpr int WS_FP = WS_HR + 16384;
constexpr int WS_FB = WS_FP + 4 * 56 * 2048;
constexpr int WS_FC = WS_FB + 114 * 2048;
constexpr int WS_END = WS_FC + 128 * 14 * 128;

constexpr int L_A = 0;
constexpr int L_DN = 65536;
constexpr int DN_PITCH = 1056;
constexpr int L_RED = L_DN + 32 * DN_PITCH + 128;
constexpr int RED_PITCH = 2304;
constexpr int L_R = L_RED + 16 * RED_PITCH;
constexpr int L_XS = L_R;
constexpr int L_Q = L_R + 10240;
constexpr int L_SM = L_Q + 1024;
constexpr int L_S4 = L_SM + 512;
constexpr int L_FAC = L_S4 + 128;
constexpr int L_FLAG = L_FAC + 256;
constexpr int F_Q = L_FLAG, F_ST = L_FLAG + 4, F_PA = L_FLAG + 8, F_PB = L_FLAG + 12, F_A = L_FLAG + 16, F_C = L_FLAG + 20,
              F_XN = L_FLAG + 24, F_KI = L_FLAG + 28, F_VI = L_FLAG + 32, F_PUB = L_FLAG + 36, F_WI = L_FLAG + 40,
              F_WO = L_FLAG + 44, L_TAG = L_FLAG + 48;
constexpr int L_DUMP = L_FLAG + 64;
constexpr int LDS_BYTES = L_DUMP + 1024;
static_assert(LDS_BYTES <= 163840 && LDS_BYTES > 81920, "one workgroup per CU");

__device__ __forceinline__ float bflo(unsigned u) { return __uint_as_float(u << 16); }
__device__ __forceinline__ float bfhi(unsigned u) { return __uint_as_float(u & 0xffff0000u); }
__device__ __forceinline__ unsigned pk2(float a, float b) {
  v2f f = {a, b};
  return __builtin_bit_cast(unsigned, __builtin_convertvector(f, v2bf));
}
__device__ __forceinline__ float rb(float a) { return bflo(pk2(a, a)); }
__device__ __forceinline__ v4f asf(v4u v) { return __builtin_bit_cast(v4f, v); }
template <int CTRL> __device__ __forceinline__ float dppf(float x) {
  return __int_as_float(__builtin_amdgcn_mov_dpp(__float_as_int(x), CTRL, 0xf, 0xf, true));
}
template <int CTRL> __device__ __forceinline__ unsigned dppu(unsigned x) {
  return (unsigned)__builtin_amdgcn_mov_dpp((int)x, CTRL, 0xf, 0xf, true);
}
template <int CTRL, int ROWM, int BANKM> __device__ __forceinline__ unsigned dppm(unsigned old, unsigned x) {
  return (unsigned)__builtin_amdgcn_update_dpp((int)old, (int)x, CTRL, ROWM, BANKM, false);
}
constexpr int XOR1 = 0xB1, XOR2 = 0x4E, HMIR = 0x141, ROR4 = 0x124, ROR8 = 0x128, IDENT = 0xE4;
constexpr int SHL1 = 0x101, SHL2 = 0x102, SHL4 = 0x104, SHL6 = 0x106;
__device__ __forceinline__ float swadd32(float a, float b) {
  auto s = __builtin_amdgcn_permlane32_swap(__float_as_uint(a), __float_as_uint(b), false, false);
  return __uint_as_float(s[0]) + __uint_as_float(s[1]);
}
__device__ __forceinline__ float swadd16(float a, float b) {
  auto s = __builtin_amdgcn_permlane16_swap(__float_as_uint(a), __float_as_uint(b), false, false);
  return __uint_as_float(s[0]) + __uint_as_float(s[1]);
}
__device__ __forceinline__ float xrow_sum(float x) { x = swadd16(x, x); return swadd32(x, x); }
__device__ __forceinline__ float wave_sum(float x) {
  x += dppf<XOR1>(x); x += dppf<XOR2>(x); x += dppf<HMIR>(x); x += dppf<ROR8>(x);
  return xrow_sum(x);
}
__device__ __forceinline__ float xrow_max(float x) {
  auto s = __builtin_amdgcn_permlane16_swap(__float_as_uint(x), __float_as_uint(x), false, false);
  x = fmaxf(__uint_as_float(s[0]), __uint_as_float(s[1]));
  auto t = __builtin_amdgcn_permlane32_swap(__float_as_uint(x), __float_as_uint(x), false, false);
  return fmaxf(__uint_as_float(t[0]), __uint_as_float(t[1]));
}
__device__ __forceinline__ float rows_scatter(v4f x) { return swadd16(swadd32(x.x, x.z), swadd32(x.y, x.w)); }
__device__ __forceinline__ void rows_scatter2(v4f x, v4f y, unsigned& zx, unsigned& zy) {
  float p = swadd32(x.x, x.z), q = swadd32(x.y, x.w), r = swadd32(y.x, y.z), s = swadd32(y.y, y.w);
  asm volatile("" : "+v"(p), "+v"(q), "+v"(r), "+v"(s));
  zx = __float_as_uint(swadd16(p, q)); zy = __float_as_uint(swadd16(r, s));
}
__device__ __forceinline__ v4u pack8(float z) {
  const unsigned r0 = pk2(z, dppf<SHL1>(z));
  return v4u{r0, dppu<SHL2>(r0), dppu<SHL4>(r0), dppu<SHL6>(r0)};
}
__device__ __forceinline__ v4f mfma(v4u a, v4u b, v4f c) {
  return __builtin_amdgcn_mfma_f32_16x16x32_bf16(__builtin_bit_cast(v8bf, a), __builtin_bit_cast(v8bf, b), c, 0, 0, 0);
}
__device__ __forceinline__ v4f mfma0(v4u a, v4u b) { return mfma(a, b, v4f{0.f, 0.f, 0.f, 0.f}); }
__device__ __forceinline__ rsrc_t mkrs(const void* p, unsigned bytes) {
  return __builtin_amdgcn_make_buffer_rsrc((void*)p, 0, bytes, 0x00020000);
}
#define PIN(x) asm volatile("" : "+v"(x))

__device__ __forceinline__ void bar() { asm volatile("s_waitcnt lgkmcnt(0)\n\ts_barrier" ::: "memory"); }
template <bool SLEEP = true> __device__ __forceinline__ void lds_wait(unsigned p, unsigned want) {
#pragma nounroll
  for (int spin = 0; spin < LDS_CAP; ++spin) {
    if ((unsigned)__builtin_amdgcn_readfirstlane(*(volatile LAS unsigned*)(size_t)p) >= want) break;
    if (SLEEP) __builtin_amdgcn_s_sleep(1);
  }
  asm volatile("" ::: "memory");
}
__device__ __forceinline__ void lds_arrive(unsigned p) {
  asm volatile("s_mov_b64 exec, 1\n\tds_add_u32 %0, %1\n\ts_mov_b64 exec, -1" :: "v"(p), "v"(1u) : "memory");
}

__device__ __forceinline__ void dma_nt(const char* sbase, unsigned voff, unsigned lds_dst) {
  unsigned keep;
  asm volatile("s_mov_b32 %0, m0\n\ts_mov_b32 m0, %3\n\ts_nop 0\n\tglobal_load_lds_dwordx4 %1, %2 nt\n\ts_mov_b32 m0, %0"
               : "=&s"(keep) : "v"(voff), "s"(sbase), "s"(lds_dst) : "memory");
}
__device__ __forceinline__ void dma_l2(const char* sbase, unsigned voff, unsigned lds_dst) {
  unsigned keep;
  asm volatile("s_mov_b32 %0, m0\n\ts_mov_b32 m0, %3\n\ts_nop 0\n\tglobal_load_lds_dwordx4 %1, %2 sc1\n\ts_mov_b32 m0, %0"
               : "=&s"(keep) : "v"(voff), "s"(sbase), "s"(lds_dst) : "memory");
}
__device__ __forceinline__ v4u trfrag(unsigned p, int second) {
  const v2u lo = __builtin_bit_cast(v2u, __builtin_amdgcn_ds_read_tr16_b64_v4i16((LAS v4s*)(size_t)p));
  const v2u hi = __builtin_bit_cast(v2u, __builtin_amdgcn_ds_read_tr16_b64_v4i16((LAS v4s*)(size_t)(p + second)));
  return v4u{lo.x, lo.y, hi.x, hi.y};
}
template <typename T> __device__ __forceinline__ LAS T& lds(unsigned p) { return *(LAS T*)(size_t)p; }
__device__ __forceinline__ v4f sum2(unsigned p) { return lds<v4f>(p) + lds<v4f>(p + 1024); }

template <int BYTE> __device__ __forceinline__ unsigned tagw(float f, unsigned tag) {
  return __builtin_amdgcn_perm(__float_as_uint(f), tag, 0x07060500u + BYTE);
}
__device__ __forceinline__ unsigned or4(v4u v) { return (v.x | v.y | v.z) | v.w; }
struct Quad { v4u a, b, c, d; };
struct Src { const char *a, *b, *c, *d; };
#define Q4OPS(q) "+v"(q.a), "+v"(q.b), "+v"(q.c), "+v"(q.d)
#define S4OPS(p) "s"(p.a), "s"(p.b), "s"(p.c), "s"(p.d)
#define Q4LOADS "s_wqm_b64 exec, %9\n\tglobal_load_dwordx4 %0, %4, %5 sc1\n\tglobal_load_dwordx4 %1, %4, %6 sc1\n\t" \
                "s_wqm_b64 exec, %10\n\tglobal_load_dwordx4 %2, %4, %7 sc1\n\tglobal_load_dwordx4 %3, %4, %8 sc1\n\ts_mov_b64 exec, -1"
__device__ __forceinline__ void quad_issue(Quad& q, unsigned off, const Src& p, u64 m1) {
  asm volatile(Q4LOADS : Q4OPS(q) : "v"(off), S4OPS(p), "s"(~0ull), "s"(m1) : "memory", "scc");
}
__device__ __forceinline__ void quad_hinted(Quad& q, unsigned off, const Src& p, const char* fp, u64 t64) {
  asm volatile(
      "s_mov_b64 s[88:89], -1\n\ts_mov_b64 s[90:91], -1\n\ts_movk_i32 s92, %11\n"
      "1:\n\ts_load_dwordx4 s[76:79], %9, 0x0 glc\n\ts_load_dwordx4 s[80:83], %9, 0x80 glc\n\ts_waitcnt lgkmcnt(0)\n\t"
      "s_xor_b64 s[76:77], s[76:77], %10\n\ts_xor_b64 s[78:79], s[78:79], %10\n\ts_xor_b64 s[80:81], s[80:81], %10\n\ts_xor_b64 s[82:83], s[82:83], %10\n\t"
      "s_or_b64 s[84:85], s[76:77], s[78:79]\n\ts_or_b64 s[86:87], s[80:81], s[82:83]\n\ts_or_b64 s[84:85], s[84:85], s[86:87]\n\t"
      "s_cbranch_scc0 2f\n\t"
      "s_cmp_eq_u32 s76, 0\n\ts_cselect_b32 s84, 0xffff, 0\n\ts_cmp_eq_u32 s77, 0\n\ts_cselect_b32 s76, 0xffff0000, 0\n\ts_or_b32 s84, s84, s76\n\t"
      "s_cmp_eq_u32 s78, 0\n\ts_cselect_b32 s85, 0xffff, 0\n\ts_cmp_eq_u32 s79, 0\n\ts_cselect_b32 s76, 0xffff0000, 0\n\ts_or_b32 s85, s85, s76\n\t"
      "s_cmp_eq_u32 s80, 0\n\ts_cselect_b32 s86, 0xffff, 0\n\ts_cmp_eq_u32 s81, 0\n\ts_cselect_b32 s76, 0xffff0000, 0\n\ts_or_b32 s86, s86, s76\n\t"
      "s_cmp_eq_u32 s82, 0\n\ts_cselect_b32 s87, 0xffff, 0\n\ts_cmp_eq_u32 s83, 0\n\ts_cselect_b32 s76, 0xffff0000, 0\n\ts_or_b32 s87, s87, s76\n\t"
      "s_and_b64 exec, s[84:85], s[88:89]\n\ts_cbranch_execz 3f\n\t"
      "global_load_dwordx4 %0, %4, %5 sc1\n\tglobal_load_dwordx4 %1, %4, %6 sc1\n\ts_andn2_b64 s[88:89], s[88:89], exec\n"
      "3:\n\ts_and_b64 exec, s[86:87], s[90:91]\n\ts_cbranch_execz 4f\n\t"
      "global_load_dwordx4 %2, %4, %7 sc1\n\tglobal_load_dwordx4 %3, %4, %8 sc1\n\ts_andn2_b64 s[90:91], s[90:91], exec\n"
      "4:\n\ts_sub_u32 s92, s92, 1\n\ts_cmp_lg_u32 s92, 0\n\ts_cbranch_scc1 1b\n"
      "2:\n\ts_mov_b64 exec, s[88:89]\n\tglobal_load_dwordx4 %0, %4, %5 sc1\n\tglobal_load_dwordx4 %1, %4, %6 sc1\n\t"
      "s_mov_b64 exec, s[90:91]\n\tglobal_load_dwordx4 %2, %4, %7 sc1\n\tglobal_load_dwordx4 %3, %4, %8 sc1\n\t"
      "s_mov_b64 exec, -1"
      : Q4OPS(q) : "v"(off), S4OPS(p), "s"(fp), "s"(t64), "n"(HINT_CAP)
      : "memory", "scc", "s76", "s77", "s78", "s79", "s80", "s81", "s82", "s83", "s84", "s85", "s86", "s87", "s88", "s89", "s90", "s91", "s92");
}
__device__ __forceinline__ void quad_wait(Quad& q) { asm volatile("s_waitcnt vmcnt(0)" : Q4OPS(q) :: "memory"); }
__device__ __forceinline__ u64 quad_strip(const Quad& q, Quad& e, v4u tag) {
  e.a = q.a ^ tag; e.b = q.b ^ tag; e.c = q.c ^ tag; e.d = q.d ^ tag;
  return __builtin_amdgcn_ballot_w64((((or4(e.a) | or4(e.b)) | (or4(e.c) | or4(e.d))) & 0xFFu) != 0);
}
__device__ __forceinline__ void quad_finish(Quad& q, Quad& e, v4u tag, unsigned off, const Src& p, u64 m1) {
  u64 m = quad_strip(q, e, tag);
  if (__builtin_expect(m != 0, 0)) {
#pragma nounroll
    for (int spin = 0; spin < SPIN_CAP && m != 0; ++spin) {
      asm volatile(Q4LOADS "\n\ts_waitcnt vmcnt(0)" : Q4OPS(q) : "v"(off), S4OPS(p), "s"(m), "s"(m & m1) : "memory", "scc");
      m = quad_strip(q, e, tag);
    }
  }
}

__global__ void __launch_bounds__(1024, 4)
fused_block(const char* __restrict__ xp, const char* __restrict__ kc, const char* __restrict__ vc, const char* __restrict__ wq,
            char* ws, const char* __restrict__ wo, const char* wg_, const char* wu_, const char* wd_, char* out_) {
  extern __shared__ __attribute__((aligned(16))) char smem_[];
  const unsigned lds0 = (unsigned)(size_t)(LAS char*)smem_;
  if (threadIdx.x < 12) lds<volatile unsigned>(lds0 + L_FLAG + threadIdx.x * 4) = 0u;
  bar();
  asm volatile("" :: "s"(xp), "s"(kc), "s"(vc), "s"(wq), "s"(wo), "s"(ws));
  const int tid = threadIdx.x, lane = tid & 63;
  const int w = __builtin_amdgcn_readfirstlane(tid >> 6);
  const int gq = lane >> 4, c = lane & 15;
  const int bid = blockIdx.x;
  const int g = bid & 7, r = bid >> 3, b = r >> 3, j = r & 7;
  const int og = b * 8 + g;
  const bool is_o = g & 1;
  const int bp = r & 1, nh = (r >> 1) & 7, xh = g >> 1;
  const bool is_f = !is_o && r < 28;
  const int cg = xh * 14 + (r >> 1);
  const int go = (g + 1 + (r >> 4)) & 7;
  const rsrc_t rs = mkrs(ws, WS_END);
  const size_t kvbase = ((size_t)og * 4096 + j * 512) * 256;

  if (w < 4 && !is_o) {
    __builtin_amdgcn_s_setprio(3);
    const int kq = r & 3, qh = (r >> 2) & 3;
    v4u xr;
    unsigned ep;
    asm volatile("global_load_dword %0, %2, %3 sc1\n\tglobal_load_dwordx4 %1, %4, %5"
                 : "=&v"(ep), "=&v"(xr) : "v"(0u), "s"(ws + WS_EPOCH), "v"(lane * 16), "s"(xp + w * 1024) : "memory");
#pragma unroll
    for (int i = 0; i < 8; ++i)
      dma_nt(wq + go * 1024 + qh * 256 + (size_t)(kq * 128 + w * 32 + i * 4) * 8192,
             gq * 8192 + ((c ^ (gq << 1) ^ ((i & 2) << 2)) << 4), lds0 + L_DN + (w * 8 + i) * 1024);
    asm volatile("s_waitcnt vmcnt(8)" : "+v"(xr), "+v"(ep) :: "memory");
    const unsigned t32 = ep + 0x11111111u;
    lds<v4u>(lds0 + L_XS + 4096 + w * 1024 + lane * 16) = xr;
    float xf[8];
#pragma unroll
    for (int d = 0; d < 4; ++d) { xf[2 * d] = bflo(xr[d]); xf[2 * d + 1] = bfhi(xr[d]); }
    const float mean = wave_sum(((xf[0] + xf[1]) + (xf[2] + xf[3])) + ((xf[4] + xf[5]) + (xf[6] + xf[7]))) * (1.f / 512.f);
    float vs = 0.f;
#pragma unroll
    for (int i = 0; i < 8; ++i) { const float d = xf[i] - mean; vs = fmaf(d, d, vs); }
    vs = wave_sum(vs);
    const float mb = rb(mean);
    const float sd = rb(__builtin_amdgcn_sqrtf(rb(rb(vs * (1.f / 512.f)) + 1e-5f)));
    const float rsd = __builtin_amdgcn_rcpf(sd);
#pragma unroll
    for (int d = 0; d < 4; ++d) {
      const unsigned t = pk2(xf[2 * d] - mb, xf[2 * d + 1] - mb);
      xr[d] = pk2(bflo(t) * rsd, bfhi(t) * rsd);
    }
    lds<v4u>(lds0 + L_XS + w * 1024 + lane * 16) = xr;
    asm volatile("s_waitcnt vmcnt(0)" ::: "memory");
    lds_arrive(lds0 + F_XN);
    lds_wait(lds0 + F_XN, 4u);
    v4u xn[4];
#pragma unroll
    for (int i = 0; i < 4; ++i) xn[i] = lds<v4u>(lds0 + L_XS + (c & 3) * 1024 + kq * 256 + gq * 16 + i * 64);
#pragma unroll
    for (int tt = 0; tt < 2; ++tt) {
      v4f a = {0.f, 0.f, 0.f, 0.f};
#pragma unroll
      for (int i = 0; i < 4; ++i)
        a = mfma(xn[i], trfrag(lds0 + L_DN + (32 * i + 8 * gq + (c >> 2)) * 256 + ((2 * w + tt) ^ (c >> 2) ^ ((gq & 1) << 2)) * 32 + (c & 3) * 8, 1024), a);
      unsigned u = __float_as_uint(a.x);
      u = dppm<IDENT, 2, 0xf>(u, __float_as_uint(a.y));
      u = dppm<IDENT, 4, 0xf>(u, __float_as_uint(a.z));
      u = dppm<IDENT, 8, 0xf>(u, __float_as_uint(a.w));
      lds<volatile unsigned>(lds0 + L_XS + 8192 + w * 512 + gq * 128 + tt * 64 + c * 4) = (u & ~0xFu) | ((t32 >> (4 * (c & 7))) & 0xFu);
    }
    const v4u q4 = lds<volatile v4u>(lds0 + L_XS + 8192 + w * 512 + (lane & 31) * 16);
    __builtin_amdgcn_raw_buffer_store_b128(
        q4, rs, lane < 32 ? WS_QG + ((go * 4 + (lane >> 3)) * 4 + kq) * 2048 + (qh * 128 + w * 32) * 4 + (lane & 7) * 16 : OOB, 0, AUX_SC1);
    __builtin_amdgcn_s_setprio(0);
  }
  __builtin_amdgcn_sched_barrier(0);

  v4u kf[8], vr[8];
  const int ch0 = (w >> 2) * 32 + (w & 3) + bid;
  {
    const unsigned kv = (c >> 2) * 256 + (c & 3) * 16 + gq * 64;
#pragma unroll
    for (int i = 0; i < 8; ++i)
      asm volatile("global_load_dwordx4 %0, %1, %2 nt" : "=v"(kf[i]) : "v"(kv), "s"(kc + kvbase + ((ch0 + 4 * i) & 127) * 1024) : "memory");
  }
  lds_arrive(lds0 + F_KI);
  if (w >= 12) {
    __builtin_amdgcn_s_setprio(2);
    const unsigned qo = WS_QG + ((g * 4 + b) * 4 + (lane >> 5)) * 2048 + (w & 3) * 512 + (lane & 31) * 16;
    v4u q0, q1, p0, p1;
    unsigned t32;
    lds_wait(lds0 + F_KI, 16u);
    asm volatile("global_load_dword %2, %6, %5 sc1\n\tglobal_load_dwordx4 %0, %3, %5 sc1\n\tglobal_load_dwordx4 %1, %4, %5 sc1\n\ts_waitcnt vmcnt(0)"
                 : "=&v"(q0), "=&v"(q1), "=&v"(t32) : "v"(qo), "v"(qo + 4096), "s"(ws), "v"((unsigned)WS_EPOCH) : "memory");
    t32 += 0x11111111u;
    if (lane == 0) lds<unsigned>(lds0 + L_TAG) = t32;
    const unsigned tq = t32 >> (16 * (lane & 1));
    const v4u t4 = {tq & 0xFu, (tq >> 4) & 0xFu, (tq >> 8) & 0xFu, (tq >> 12) & 0xFu};
#pragma nounroll
    for (int spin = 0; spin < SPIN_CAP; ++spin) {
      p0 = q0 ^ t4; p1 = q1 ^ t4;
      const u64 m = __builtin_amdgcn_ballot_w64(((or4(p0) | or4(p1)) & 0xFu) != 0);
      if (m == 0) break;
      asm volatile("s_wqm_b64 exec, %5\n\tglobal_load_dwordx4 %0, %2, %4 sc1\n\tglobal_load_dwordx4 %1, %3, %4 sc1\n\ts_mov_b64 exec, -1\n\ts_waitcnt vmcnt(0)"
                   : "+v"(q0), "+v"(q1) : "v"(qo), "v"(qo + 4096), "s"(ws), "s"(m) : "memory", "scc");
    }
    const v4f qs = asf(p0) + asf(p1);
    lds<unsigned>(lds0 + L_Q + (w & 3) * 256 + (lane & 31) * 8 + (lane >> 5) * 4) = pk2(swadd32(qs.x, qs.z), swadd32(qs.y, qs.w));
    lds_arrive(lds0 + F_Q);
  }
  v4q la;
  asm volatile("s_load_dwordx8 %0, %1, 0x30\n\ts_waitcnt lgkmcnt(0)" : "=&s"(la) : "s"(__builtin_amdgcn_kernarg_segment_ptr()) : "memory");
  const char* wg = (const char*)la.x;
  const char* wu = (const char*)la.y;
  const char* wd = (const char*)la.z;
  char* outp = (char*)la.w;

  const rsrc_t rv = mkrs(vc + kvbase, 131072);
  if (w < 12) {
    lds_wait(lds0 + F_KI, 16u);
#pragma unroll
    for (int i = 0; i < 8; ++i) vr[i] = __builtin_amdgcn_raw_buffer_load_b128(rv, lane * 16, ((ch0 + 4 * i) & 127) * 1024, AUX_NT);
    __builtin_amdgcn_sched_barrier(0);
    lds_arrive(lds0 + F_VI);
  } else {
#pragma unroll
    for (int i = 0; i < 8; ++i) asm volatile("" : "=v"(vr[i]));
  }
  asm volatile("s_waitcnt vmcnt(8)" : "+v"(kf[0]), "+v"(kf[1]), "+v"(kf[2]), "+v"(kf[3]), "+v"(kf[4]), "+v"(kf[5]), "+v"(kf[6]), "+v"(kf[7]) :: "memory");
  lds_wait(lds0 + F_Q, 4u);
  const unsigned tagf = __builtin_amdgcn_readfirstlane(lds<unsigned>(lds0 + L_TAG));
  const v4u tag = {tagf & 0xFFu, (tagf >> 8) & 0xFFu, (tagf >> 16) & 0xFFu, tagf >> 24};

  v4u pm[4];
  {
    const v4u qf = lds<v4u>(lds0 + L_Q + (c >> 2) * 256 + (4 * gq + (c & 3)) * 16);
    float sc[8];
#pragma unroll
    for (int i = 0; i < 8; ++i) {
      const v4f a = mfma0(kf[i], qf);
      sc[i] = (dppf<0x00>(a.x) + dppf<0x55>(a.y)) + (dppf<0xAA>(a.z) + dppf<0xFF>(a.w));
    }
#pragma unroll
    for (int d = 0; d < 4; ++d) {
      const unsigned u1 = pk2(sc[2 * d], sc[2 * d + 1]);
      const unsigned u2 = pk2(bflo(u1) * 0.08838834764831845f, bfhi(u1) * 0.08838834764831845f);
      sc[2 * d] = bflo(u2); sc[2 * d + 1] = bfhi(u2);
    }
    float mw = fmaxf(fmaxf(fmaxf(sc[0], sc[1]), fmaxf(sc[2], sc[3])), fmaxf(fmaxf(sc[4], sc[5]), fmaxf(sc[6], sc[7])));
    mw = xrow_max(mw);
    float lw = 0.f;
#pragma unroll
    for (int i = 0; i < 8; ++i) { sc[i] = __builtin_amdgcn_exp2f((sc[i] - mw) * LOG2E); lw += sc[i]; }
    lw = xrow_sum(lw);
    if (gq == 0 && (c & 3) == 0) lds<v2f>(lds0 + L_SM + ((c >> 2) * 16 + w) * 8) = v2f{mw, lw};
    lds_arrive(lds0 + F_ST);
    v4u pb;
#pragma unroll
    for (int t = 0; t < 4; ++t) pb[t] = pk2(sc[2 * t], sc[2 * t + 1]);
#pragma unroll
    for (int e4 = 0; e4 < 4; ++e4) {
      const bool on = (c & 3) == e4;
      pm[e4] = v4u{on ? pb.x : 0u, on ? pb.y : 0u, on ? pb.z : 0u, on ? pb.w : 0u};
      PIN(pm[e4]);
    }
  }

  const int u8 = (w & 3) + 4 * (w >> 3);
  const bool dataw = (w & 4) == 0;
  unsigned off = gq * 2048 + (c >> 3) * (is_o ? 131072 : 16384) + u8 * 256 + (c & 7) * 16, rslot = lds0 + L_R + u8 * 1024 + lane * 16;
  unsigned tr0, tr1, tr2, tr3, rsrc, so, sslot;
  if (is_o) {
    const int swz = (c >> 3) | ((gq & 1) << 1);
    const unsigned tb = lds0 + L_A + u8 * 8192 + (8 * gq + (c >> 2)) * 128 + (c & 3) * 8;
    tr0 = tb + swz * 32; tr1 = tb + (swz ^ 1) * 32; tr2 = tb + (swz ^ 2) * 32; tr3 = tb + (swz ^ 3) * 32;
    rsrc = lds0 + L_R + gq * 2048 + (w & 1) * 512 + c * 16;
    so = WS_HP + ((2 * bp + (w & 1)) * 8 + (r >> 4) * 4 + xh) * 2048 + nh * 256 + lane * 4;
    sslot = lds0 + L_FAC + ((c >> 3) * 32 + gq * 4 + (u8 >> 1)) * 4;
  } else {
    const unsigned tb = lds0 + L_A + u8 * 8192 + (4 * gq + (c >> 2)) * 64 + (c & 3) * 8;
    tr0 = tb + (gq & 1) * 32; tr1 = tb + ((gq & 1) ^ 1) * 32;
    tr2 = lds0 + L_DN + (16 * (gq & 1) + 8 * (gq >> 1) + (c >> 2)) * DN_PITCH + (gq & 1) * 128 + (w & 3) * 256 + (c & 3) * 8;
    tr3 = lds0 + L_S4 + ((2 * (c >> 3) + (gq & 1)) * 8 + (c & 7)) * 4;
    rsrc = lds0 + L_R + gq * 2048 + (c >> 3) * 512 + (c & 7) * 16;
    so = (gq & 1) ? OOB : WS_FP + ((2 * bp + (gq >> 1)) * 56 + cg) * 2048 + (w & 3) * 512 + c * 16;
    sslot = (lane == 0 || lane == 4 || lane == 36 || lane == 40) ? lds0 + L_S4 + (gq + ((lane >> 2) & 1)) * 32 + u8 * 4 : lds0 + L_DUMP + lane * 4;
  }
  const unsigned xrp = lds0 + L_XS + 4096 + (2 * bp + (c >> 3)) * 1024 + (64 * u8 + 4 * (c & 7) + gq) * 2;
  float xa = bflo(lds<unsigned short>(xrp)), xb = bflo(lds<unsigned short>(xrp + 64));
  PIN(xa); PIN(xb); PIN(off); PIN(tr0); PIN(tr1); PIN(tr2); PIN(tr3); PIN(rsrc); PIN(so); PIN(sslot); PIN(rslot);

  if (w >= 12) {
    lds_wait(lds0 + F_VI, 12u);
#pragma unroll
    for (int i = 0; i < 8; ++i) vr[i] = __builtin_amdgcn_raw_buffer_load_b128(rv, lane * 16, ((ch0 + 4 * i) & 127) * 1024, AUX_NT);
    __builtin_amdgcn_sched_barrier(0);
    lds_arrive(lds0 + F_VI);
  }

  {
    unsigned rp = lds0 + L_RED + w * RED_PITCH + (36 * (c >> 2) + 8 * gq + (c & 3)) * 16;
    unsigned fp = lds0 + (w < 12 ? F_PA : F_PB);
    PIN(rp); PIN(fp);
    v4u vf[8];
#pragma unroll
    for (int t = 0; t < 4; ++t) {
#pragma unroll
      for (int e = 0; e < 8; ++e)
        vf[e][t] = __builtin_amdgcn_perm(vr[2 * t + 1][e >> 1], vr[2 * t][e >> 1], (e & 1) ? 0x07060302u : 0x05040100u);
      if (t < 3) __builtin_amdgcn_sched_barrier(0);
    }
    v4f acc0 = {0.f, 0.f, 0.f, 0.f}, acc1 = acc0;
#pragma unroll
    for (int e4 = 0; e4 < 4; ++e4) {
      acc0 = mfma(vf[e4], pm[e4], acc0);
      acc1 = mfma(vf[4 + e4], pm[e4], acc1);
    }
    lds<v4f>(rp) = acc0;
    lds<v4f>(rp + 64) = acc1;
    lds_arrive(fp);
    __builtin_amdgcn_s_setprio(0);
  }

  if (w < 4) {
    const int hf = lane >> 5;
    __builtin_amdgcn_s_setprio(3);
    lds_wait(lds0 + F_ST, 16u);
    if (w == 0) {
      const v2f st = lds<v2f>(lds0 + L_SM + ((lane & 3) * 16 + (lane >> 2)) * 8);
      float M = st.x;
      M = fmaxf(M, dppf<ROR4>(M)); M = fmaxf(M, dppf<ROR8>(M)); M = xrow_max(M);
      float L = st.y * __builtin_amdgcn_exp2f((st.x - M) * LOG2E);
      L += dppf<ROR4>(L); L += dppf<ROR8>(L); L = xrow_sum(L);
      __builtin_amdgcn_raw_buffer_store_b128(v4u{__float_as_uint(M), tagf, __float_as_uint(L), tagf}, rs,
                                             lane < 4 ? WS_ST + (og * 8 + j) * 128 + lane * 16 : OOB, 0, AUX_SC1);
    }
    float M = lds<float>(lds0 + L_SM + (w * 16 + c) * 8);
    M = fmaxf(M, dppf<XOR1>(M)); M = fmaxf(M, dppf<XOR2>(M)); M = fmaxf(M, dppf<HMIR>(M)); M = fmaxf(M, dppf<ROR8>(M));
    float f[8];
#pragma unroll
    for (int i = 0; i < 8; ++i) {
      f[i] = __builtin_amdgcn_exp2f((lds<float>(lds0 + L_SM + (w * 16 + 2 * i + hf) * 8) - M) * LOG2E);
      PIN(f[i]);
    }
    const unsigned rp = lds0 + L_RED + hf * RED_PITCH + (36 * w + (lane & 31)) * 16;
    unsigned oo = WS_OP + (og * 8 + j) * 2048 + (32 * w + (lane & 31)) * 16 + hf * 8;
    unsigned tg = tagf >> (16 * hf);
    PIN(oo); PIN(tg);
    lds_wait(lds0 + F_PA, 12u);
    v4f s = {0.f, 0.f, 0.f, 0.f};
#pragma unroll
    for (int i = 0; i < 6; ++i) s += lds<v4f>(rp + 2 * i * RED_PITCH) * f[i];
    PIN(s);
    v4f t0, t1;
    __builtin_amdgcn_s_setprio(0);
#pragma nounroll
    for (int spin = 0; spin < LDS_CAP; ++spin) {
      const unsigned cnt = lds<volatile unsigned>(lds0 + F_PB);
      t0 = lds<volatile v4f>(rp + 12 * RED_PITCH);
      t1 = lds<volatile v4f>(rp + 14 * RED_PITCH);
      if (__builtin_amdgcn_readfirstlane(cnt) == 4u) break;
    }
    s += t0 * f[6];
    s += t1 * f[7];
    __builtin_amdgcn_raw_buffer_store_b64(v2u{tagw<0>(swadd32(s.x, s.z), tg), tagw<1>(swadd32(s.y, s.w), tg)}, rs, oo, 0, AUX_SC1);
    lds_arrive(lds0 + F_PUB);
  } else if (w < 8) {
    const int u = w & 3;
    lds_wait(lds0 + F_VI, 16u);
    if (is_o) {
      const char* src = wo + (size_t)(go * 512 + u * 8) * 1024 + nh * 128;
      const unsigned sv = (lane >> 3) * 1024 + (((lane & 7) ^ (((lane >> 4) & 1) << 1) ^ ((u & 1) << 2)) << 4);
#pragma unroll
      for (int i = 0; i < 16; ++i) dma_nt(src + i * 32768, sv, lds0 + L_A + u * 1024 + i * 4096);
      lds_arrive(lds0 + F_WI);
    } else {
      const unsigned pv = min(lane, 55) * 16;
#pragma nounroll
      for (int k = 0; k < 8; ++k) {
        const char* p = (k < 4 ? wg : wu) + (size_t)(16 * r + 4 * u + (k & 3)) * 3584 + xh * 896;
        dma_nt(p, pv, lds0 + L_DUMP);
        dma_l2(p, pv, lds0 + L_DUMP);
      }
      lds_wait(lds0 + F_PUB, 4u);
      if (is_f) {
#pragma unroll
        for (int i = 0; i < 8; ++i) dma_nt(wd + cg * 32768 + (8 * u + i) * 1024, lane * 16, lds0 + L_DN + (8 * u + i) * DN_PITCH + (u >> 1) * 128);
        const unsigned gv = (4 * (c >> 2) + gq) * 3584 + (((lane & 3) ^ ((gq & 1) << 1)) << 4);
#pragma unroll
        for (int i = 0; i < 16; ++i) {
          const int kb = 4 * u + ((i >> 1) & 3);
          if (i == 8) lds_arrive(lds0 + F_WI);
          dma_l2((i < 8 ? wg : wu) + (size_t)(32 * kb + 16 * (i & 1)) * 3584 + cg * 64, gv, lds0 + L_A + kb * 4096 + (i >> 3) * 2048 + (i & 1) * 1024);
        }
      } else {
        lds_arrive(lds0 + F_WI);
      }
    }
    asm volatile("s_waitcnt vmcnt(0)" ::: "memory");
    lds_arrive(lds0 + F_WO);
  } else if (w == 9) {
    const char* pc;
    int e0, e1;
    asm volatile("s_getpc_b64 %0\n.Lfb_here:\n\ts_mov_b32 %1, .Lfb_end_o-.Lfb_here\n\ts_mov_b32 %2, .Lfb_end_f-.Lfb_here" : "=s"(pc), "=s"(e0), "=s"(e1));
    dma_l2((const char*)((size_t)pc & ~(size_t)15), min((r & 15) * 1024 + lane * 16, max(max(e0, e1) - 16, 0) & ~15), lds0 + L_DUMP);
  } else if (w == 13 && is_o) {
    __builtin_amdgcn_s_setprio(3);
    v4u st;
#pragma nounroll
    for (int spin = 0; spin < SPIN_CAP; ++spin) {
      asm volatile("" ::: "memory");
      st = __builtin_amdgcn_raw_buffer_load_b128(rs, WS_ST + (((2 * bp + (lane >> 5)) * 8 + go) * 8 + ((lane >> 2) & 7)) * 128 + (lane & 3) * 16, 0, AUX_SC1);
      if (__builtin_amdgcn_ballot_w64(((st.y ^ tagf) | (st.w ^ tagf)) != 0) == 0) break;
      __builtin_amdgcn_s_sleep(2);
    }
    const float m = __uint_as_float(st.x);
    float M = fmaxf(m, dppf<ROR4>(m)); M = fmaxf(M, dppf<ROR8>(M));
    auto ms = __builtin_amdgcn_permlane16_swap(__float_as_uint(M), __float_as_uint(M), false, false);
    M = fmaxf(__uint_as_float(ms[0]), __uint_as_float(ms[1]));
    const float wt = __builtin_amdgcn_exp2f((m - M) * LOG2E);
    float L = __uint_as_float(st.z) * wt;
    L += dppf<ROR4>(L); L += dppf<ROR8>(L); L = swadd16(L, L);
    lds<float>(lds0 + L_FAC + lane * 4) = wt * __builtin_amdgcn_rcpf(L);
    lds_arrive(lds0 + F_WO);
    __builtin_amdgcn_s_setprio(0);
  }

  if (is_o) {
    if (dataw) {
      const char* pa = ws + WS_OP + (2 * bp * 8 + go) * 16384;
      Src p = {pa, pa + 128, pa + 8192, pa + 128 + 8192};
      unsigned fa = lds0 + F_A;
      asm volatile("" : "+s"(p.a), "+s"(p.b), "+s"(p.c), "+s"(p.d), "+v"(fa));
      Quad q, e;
      asm volatile("" : "=v"(q.a), "=v"(q.b), "=v"(q.c), "=v"(q.d));
      lds_wait(lds0 + F_WI, 4u);
      quad_issue(q, off, p, ~0ull);
      lds_wait<false>(lds0 + F_WO, 5u);
      const float f0 = lds<float>(sslot), f1 = lds<float>(sslot + 64);
      v4u wf[8];
#pragma unroll
      for (int t = 0; t < 8; ++t) wf[t] = trfrag(((t & 3) == 0 ? tr0 : (t & 3) == 1 ? tr1 : (t & 3) == 2 ? tr2 : tr3) + (t >> 2) * 4096, 512);
      quad_wait(q);
      quad_finish(q, e, tag, off, p, ~0ull);
      lds_arrive(fa);
      __builtin_amdgcn_sched_barrier(0);
      unsigned z0, z1;
      rows_scatter2(asf(e.a) * f0 + asf(e.c) * f1, asf(e.b) * f0 + asf(e.d) * f1, z0, z1);
      const v4u oa = pack8(__uint_as_float(z0)), ob = pack8(__uint_as_float(z1));
      v4f m[4];
#pragma unroll
      for (int t = 0; t < 4; ++t) m[t] = mfma0(oa, wf[t]);
#pragma unroll
      for (int t = 0; t < 4; ++t) m[t] = mfma(ob, wf[4 + t], m[t]);
      asm volatile("" : "+v"(m[0]), "+v"(m[1]), "+v"(m[2]), "+v"(m[3]));
      lds<v4f>(rslot) = v4f{m[0].x, m[1].x, m[2].x, m[3].x};
    } else if (w == 12) {
      unsigned fo = lane < 57 ? WS_FB + ((bp * 57 + lane) * 16 + nh * 2 + (r >> 4)) * 128 + xh * 4 : OOB;
      PIN(fo);
      lds_wait<false>(lds0 + F_A, 8u);
      __builtin_amdgcn_raw_buffer_store_b32(tagf, rs, fo, 0, AUX_SC1);
    }
    if ((w >> 1) != 2) {
      bar();
      const char* fp = ws + WS_FC + ((xh * 32 + r) * 14 + (w < 4 ? w : w - 2)) * 128;
#pragma nounroll
      for (int spin = 0; spin < HINT_CAP; ++spin) {
        v4u f;
        asm volatile("s_load_dwordx4 %0, %1, 0x0 glc\n\ts_waitcnt lgkmcnt(0)" : "=&s"(f) : "s"(fp) : "memory");
        if (((f.x ^ tagf) | (f.y ^ tagf) | (f.z ^ tagf) | (f.w ^ tagf)) == 0) break;
      }
      lds_arrive(lds0 + F_C);
    } else {
      unsigned tg = tagf >> (8 * (lane & 3));
      PIN(tg);
      bar();
      __builtin_amdgcn_raw_buffer_store_b32(tagw<0>(rows_scatter(sum2(rsrc)), tg), rs, so, 0, AUX_SC1);
      if (w == 4) {
        const int fcol = 64 * (r >> 2) + 16 * gq + 4 * (r & 3) + (c & 3);
        const rsrc_t ro = mkrs(outp, 4096);
        unsigned oo = c < 4 ? xh * 1024 + fcol * 2 : OOB;
        unsigned fo = (lane >> 2) * 2048 + r * 64 + (lane & 3) * 16;
        const char* pf = ws + WS_FP + xh * 56 * 2048;
        Src p = {pf, pf + 16 * 2048, pf + 32 * 2048, pf + 44 * 2048};
        asm volatile("" : "+s"(p.a), "+s"(p.b), "+s"(p.c), "+s"(p.d));
        Quad q, e;
        asm volatile("" : "=v"(q.a), "=v"(q.b));
        q.c = tag; q.d = tag;
        PIN(oo); PIN(fo); PIN(q.c); PIN(q.d);
        v2u h0;
#pragma nounroll
        for (int spin = 0; spin < SPIN_CAP; ++spin) {
          asm volatile("" ::: "memory");
          h0 = __builtin_amdgcn_raw_buffer_load_b64(rs, WS_HR + (xh * 512 + fcol) * 8, 0, AUX_SC1);
          if (__builtin_amdgcn_ballot_w64((((h0.x ^ tagf) & 0xFFFFu) | (h0.y ^ tagf)) != 0) == 0) break;
        }
        float hv = bfhi(h0.x);
        PIN(hv);
        lds_wait<false>(lds0 + F_C, 14u);
        quad_issue(q, fo, p, 0xFFFFFFFFFFFFull);
        quad_wait(q);
        quad_finish(q, e, tag, fo, p, 0xFFFFFFFFFFFFull);
        float s = rows_scatter((asf(e.a) + asf(e.b)) + (asf(e.c) + asf(e.d)));
        s += dppf<ROR4>(s); s += dppf<ROR8>(s);
        const float o = hv + rb(s);
        __builtin_amdgcn_raw_buffer_store_b16((unsigned short)pk2(o, o), ro, oo, 0, 0);
        asm volatile(".Lfb_end_o:" ::: "memory");
      } else if (bid == 1) {
#pragma nounroll
        for (int spin = 0; spin < SPIN_CAP; ++spin) {
          asm volatile("" ::: "memory");
          const v2u h0 = __builtin_amdgcn_raw_buffer_load_b64(rs, WS_HR + (lane & 3) * 4096 + (lane & 60) * 8, 0, AUX_SC1);
          if (__builtin_amdgcn_ballot_w64(h0.y != tagf) == 0) break;
          __builtin_amdgcn_s_sleep(8);
        }
        if (lane == 0) __hip_atomic_store((GAS unsigned*)(ws + WS_EPOCH), tagf, __ATOMIC_RELAXED, __HIP_MEMORY_SCOPE_AGENT);
      }
    }
  } else if (is_f || cg == 14) {
    if (dataw) {
      const char* pa = ws + WS_HP + 2 * bp * 16384;
      const Src p = {pa, pa + 128, pa + 8192, pa + 128 + 8192};
      const char* fp = ws + WS_FB + ((bp * 57 + (is_f ? cg : 56)) * 16 + 2 * u8) * 128;
      unsigned fc = lds0 + F_C;
      v4u ha = {ONES, ONES, ONES, ONES}, hb = ha;
      PIN(ha); PIN(hb); PIN(fc);
      Quad q, e;
      asm volatile("" : "=v"(q.a), "=v"(q.b), "=v"(q.c), "=v"(q.d));
      lds_wait(lds0 + F_WI, 4u);
      quad_hinted(q, off, p, fp, ((u64)tagf << 32) | tagf);
      lds_wait<false>(lds0 + F_WO, 4u);
      v4u gf[8];
#pragma unroll
      for (int t = 0; t < 8; ++t) gf[t] = trfrag(((t & 1) ? tr1 : tr0) + ((t >> 1) & 1) * 2048 + (t >> 2) * 4096, 1024);
      quad_wait(q);
      lds_arrive(fc);
      quad_finish(q, e, tag, off, p, ~0ull);
      unsigned z0, z1;
      rows_scatter2(asf(e.a) + asf(e.c), asf(e.b) + asf(e.d), z0, z1);
      const unsigned zz = pk2(__uint_as_float(z0), __uint_as_float(z1));
      const float za = xa + bflo(zz), zb = xb + bfhi(zz);
      const unsigned ra = pk2(za, dppf<SHL1>(za)), rb2 = pk2(zb, dppf<SHL1>(zb));
      if (__builtin_expect(is_f, 1)) {
        ha = v4u{dppm<IDENT, 0xf, 0xd>(ha.x, ra), dppm<SHL2, 0xf, 0xd>(ha.y, ra), dppm<SHL4, 0xf, 0xd>(ha.z, ra), dppm<SHL6, 0xf, 0xd>(ha.w, ra)};
        v4f m[5];
        m[4] = mfma0(ha, ha);
        __builtin_amdgcn_sched_barrier(0);
        hb = v4u{dppm<IDENT, 0xf, 0xd>(hb.x, rb2), dppm<SHL2, 0xf, 0xd>(hb.y, rb2), dppm<SHL4, 0xf, 0xd>(hb.z, rb2), dppm<SHL6, 0xf, 0xd>(hb.w, rb2)};
#pragma unroll
        for (int t = 0; t < 4; ++t) m[t] = mfma0(ha, gf[t]);
        m[4] = mfma(hb, hb, m[4]);
#pragma unroll
        for (int t = 0; t < 4; ++t) m[t] = mfma(hb, gf[4 + t], m[t]);
        asm volatile("" : "+v"(m[0]), "+v"(m[1]), "+v"(m[2]), "+v"(m[3]), "+v"(m[4]));
        lds<float>(sslot) = m[4].x;
        lds<v4f>(rslot) = v4f{m[0].x, m[1].x, m[2].x, m[3].x};
        bar();
      } else {
        const int ho = WS_HR + ((2 * bp + (c >> 3)) * 512 + 64 * u8 + 4 * (c & 7) + gq) * 8;
        __builtin_amdgcn_raw_buffer_store_b64(v2u{(ra << 16) | (tagf & 0xFFFFu), tagf}, rs, ho, 0, AUX_SC1);
        __builtin_amdgcn_raw_buffer_store_b64(v2u{(rb2 << 16) | (tagf & 0xFFFFu), tagf}, rs, ho + 256, 0, AUX_SC1);
      }
    } else if (is_f && w == 4) {
      unsigned fo = WS_FC + ((64 * bp + lane) * 14 + (r >> 1)) * 128 + xh * 4;
      PIN(fo);
      lds_wait<false>(lds0 + F_C, 8u);
      __builtin_amdgcn_s_sleep(C_DELAY);
      __builtin_amdgcn_raw_buffer_store_b32(tagf, rs, fo, 0, AUX_SC1);
      bar();
    } else if (is_f && w >= 12) {
      v4u df[8], sel = {0x07060500u, 0x07060501u, 0x07060502u, 0x07060503u};
      lds_wait(lds0 + F_WO, 4u);
#pragma unroll
      for (int t = 0; t < 8; ++t) df[t] = trfrag(tr2 + t * 32, 4 * DN_PITCH);
      unsigned csrc = lds0 + L_R + gq * 2048 + 256 + c * 16;
      PIN(sel); PIN(csrc);
      bar();
      float st = lds<float>(tr3);
      v4f r0 = lds<v4f>(rsrc), r1 = lds<v4f>(rsrc + 1024), r2 = lds<v4f>(rsrc + 128), r3 = lds<v4f>(rsrc + 1152);
      v4f r4 = lds<v4f>(csrc), r5 = lds<v4f>(csrc + 1024);
      __builtin_amdgcn_sched_barrier(0);
      st += dppf<XOR1>(st); st += dppf<XOR2>(st);
      __builtin_amdgcn_sched_barrier(0);
      st += dppf<HMIR>(st);
      const v4f s0 = r0 + r1, s1 = r2 + r3;
      const unsigned cs = __float_as_uint(rows_scatter(r4 + r5));
      auto cw = __builtin_amdgcn_permlane32_swap(dppm<ROR8, 0xf, 0xc>(cs, cs), dppm<ROR8, 0xf, 0x3>(cs, cs), false, false);
      auto sw = __builtin_amdgcn_permlane16_swap(__float_as_uint(st), __float_as_uint(st), false, false);
      const float mean = __uint_as_float(sw[1]) * (1.f / 512.f);
      const float var = fmaxf(__uint_as_float(sw[0]) * (1.f / 512.f) - mean * mean, 0.f);
      const float mb = rb(mean);
      const float sd = rb(__builtin_amdgcn_sqrtf(rb(rb(var) + 1e-5f)));
      const float rsd = __builtin_amdgcn_rcpf(sd);
      unsigned y0, y1;
      rows_scatter2(s0, s1, y0, y1);
      auto gu = __builtin_amdgcn_permlane32_swap(y0, y1, false, false);
      const unsigned y = pk2((__uint_as_float(gu[0]) - mb * __uint_as_float(cw[0])) * rsd, (__uint_as_float(gu[1]) - mb * __uint_as_float(cw[1])) * rsd);
      const float gv = bflo(y);
      const float sl = rb(gv * __builtin_amdgcn_rcpf(1.f + __builtin_amdgcn_exp2f(-gv * LOG2E)));
      const v4u af = pack8(sl * bfhi(y));
      v4f m[8];
#pragma unroll
      for (int t = 0; t < 8; ++t) m[t] = mfma0(af, df[t]);
      __builtin_amdgcn_sched_barrier(0);
      asm volatile("" : "+v"(m[0]), "+v"(m[1]), "+v"(m[2]), "+v"(m[3]));
      asm volatile("" : "+v"(m[4]), "+v"(m[5]), "+v"(m[6]), "+v"(m[7]));
#pragma unroll
      for (int hh = 0; hh < 2; ++hh) {
        v4u y4;
#pragma unroll
        for (int t = 0; t < 4; ++t) y4[t] = __builtin_amdgcn_perm(__float_as_uint(m[4 * hh + t].x), tagf, sel[t]);
        __builtin_amdgcn_raw_buffer_store_b128(y4, rs, so + hh * 256, 0, AUX_SC1);
      }
      asm volatile(".Lfb_end_f:" ::: "memory");
    } else if (is_f) {
      bar();
    }
  }
}

extern "C" void kernel_launch(void* const* d_in, const int* in_sizes, int n_in, void* d_out, int out_size, void* d_ws,
                              size_t ws_size, hipStream_t stream) {
  (void)hipFuncSetAttribute((const void*)fused_block, hipFuncAttributeMaxDynamicSharedMemorySize, LDS_BYTES);
  fused_block<<<256, 1024, LDS_BYTES, stream>>>((const char*)d_in[0], (const char*)d_in[1], (const char*)d_in[2], (const char*)d_in[3],
                                                (char*)d_ws, (const char*)d_in[4], (const char*)d_in[6], (const char*)d_in[7],
                                                (const char*)d_in[8], (char*)d_out);
}
```
